```python
import torch

def _softmax(x, axis=-1):
    return torch.softmax(x, dim=axis)

def _gelu(x):
    return torch.nn.functional.gelu(x, approximate='tanh')

def _silu(x):
    return torch.nn.functional.silu(x)

def _sigmoid(x):
    return torch.sigmoid(x)

def _layernorm(x, axis=-1, eps=1e-05):
    m = x.mean(dim=axis, keepdim=True)
    v = x.var(dim=axis, keepdim=True, unbiased=False)
    return (x - m) / torch.sqrt(v + eps)

def _scatter_add(tgt, idx, src, axis=0):
    return tgt.clone().index_add_(axis, idx, src)

def _segment_sum(src, seg, n):
    out = torch.zeros((n,) + src.shape[1:], dtype=src.dtype)
    return out.index_add_(0, seg, src)

def _pad(x, pw):
    flat = tuple((v for pair in reversed(pw) for v in pair))
    return torch.nn.functional.pad(x, flat)
kernel_name = 'sequential_preLN__memory_aug_recurrent_mem_tr__mixer_token_mlp__prompt'

def setup_inputs():

    _w = lambda a: (lambda t: t.to(torch.bfloat16) if t.is_floating_point() else t)(torch.as_tensor(a))

    def _A_setup():
        B, S, Dm = (1, 2048, 512)
        H, Dh = (32, 128)
        M, seg = (8, 32)
        seg = min(seg, S)
        S = S // seg * seg
        return {'x': _w((lambda *s: torch.randn(*s, dtype=torch.bfloat16) * 0.05)(B, S, Dm)), 'mem0': _w((lambda *s: torch.randn(*s, dtype=torch.bfloat16) * 0.2)(B, M, Dm)), 'Wq': _w((lambda *s: torch.randn(*s, dtype=torch.bfloat16) * 1.6)(Dm, H, Dh) * Dm ** (-0.5)), 'Wk': _w((lambda *s: torch.randn(*s, dtype=torch.bfloat16) * 1.6)(Dm, H, Dh) * Dm ** (-0.5)), 'Wv': _w((lambda *s: torch.randn(*s, dtype=torch.bfloat16) * 0.2)(Dm, H, Dh) * Dm ** (-0.5)), 'Wo': _w((lambda *s: torch.randn(*s, dtype=torch.bfloat16) * 0.2)(H, Dh, Dm) * (H * Dh) ** (-0.5)), 'seg_len': seg}
    _A_in = _A_setup()

    def _B_setup():
        B, S, D, F = (1, 2048, 512, 1792)
        return {'x': _w((lambda *s: torch.randn(*s, dtype=torch.bfloat16) * 0.05)(B, S, D)), 'w1': _w((lambda *s: torch.randn(*s, dtype=torch.bfloat16) * 0.4)(S, F) * S ** (-0.5)), 'b1': _w((lambda *s: torch.randn(*s, dtype=torch.bfloat16) * 3.2)(F) * 0.02), 'w2': _w((lambda *s: torch.randn(*s, dtype=torch.bfloat16) * 0.2)(F, S) * F ** (-0.5)), 'b2': _w((lambda *s: torch.randn(*s, dtype=torch.bfloat16) * 0.4)(S) * 0.02)}
    _B_in = _B_setup()
    return {'A_x': _A_in['x'], 'A_mem0': _A_in['mem0'], 'A_Wq': _A_in['Wq'], 'A_Wk': _A_in['Wk'], 'A_Wv': _A_in['Wv'], 'A_Wo': _A_in['Wo'], 'A_seg_len': _A_in['seg_len'], 'B_x': _B_in['x'], 'B_w1': _B_in['w1'], 'B_b1': _B_in['b1'], 'B_w2': _B_in['w2'], 'B_b2': _B_in['b2']}

def reference(A_x, A_mem0, A_Wq, A_Wk, A_Wv, A_Wo, A_seg_len, B_x, B_w1, B_b1, B_w2, B_b2):

    def _A_ref(x, mem0, Wq, Wk, Wv, Wo, seg_len):
        B, S, Dm = x.shape
        H, Dh = (Wq.shape[1], Wq.shape[2])
        M = mem0.shape[1]
        scale = Dh ** (-0.5)
        n_seg = S // seg_len
        L = M + seg_len
        idx = torch.arange(L)
        mask = (idx[:, None] < M) | (idx[:, None] >= idx[None, :])
        mem = mem0
        outs = []
        for s in range(n_seg):
            xs = x[:, s * seg_len:(s + 1) * seg_len, :].clone()
            h = torch.concatenate([mem, xs], axis=1)
            q = torch.einsum('btd,dhe->bhte', h, Wq)
            k = torch.einsum('btd,dhe->bhte', h, Wk)
            v = torch.einsum('btd,dhe->bhte', h, Wv)
            scores = torch.einsum('bhqe,bhke->bhqk', q, k) * scale
            scores = torch.where(mask[None, None], scores, -1000000000.0)
            p = _softmax(scores, axis=-1)
            o = torch.einsum('bhqk,bhke->bhqe', p, v)
            o = torch.einsum('bhte,hed->btd', o, Wo)
            mem = o[:, :M, :].clone()
            outs.append(o[:, M:, :])
        return torch.concatenate(outs, axis=1)

    def _B_ref(x, w1, b1, w2, b2):
        B, S, D = x.shape
        h = _layernorm(x, axis=-1)
        u = torch.einsum('bsd,sf->bdf', h, w1) + b1
        u = _gelu(u)
        y = torch.einsum('bdf,fs->bds', u, w2) + b2
        return (lambda x, *d: x.permute(*d))(y, 0, 2, 1)
    x = A_x
    h = x + _A_ref(_layernorm(x, axis=-1), A_mem0, A_Wq, A_Wk, A_Wv, A_Wo, A_seg_len)
    h2 = h + _B_ref(_layernorm(h, axis=-1), B_w1, B_b1, B_w2, B_b2)
    return h2
```

```cpp
#include <hip/hip_runtime.h>
#include <hip/hip_bf16.h>
typedef unsigned short bf;
__device__ __forceinline__ float b2f(bf x) { return __uint_as_float((unsigned)x << 16); }
__device__ __forceinline__ bf f2b(float f) { __bf16 b = (__bf16)f; return __builtin_bit_cast(bf, b); }
__device__ __forceinline__ float rb(float f) { return b2f(f2b(f)); }

__device__ float blksum(float v, float* sh) {
    for (int o = 32; o; o >>= 1) v += __shfl_xor(v, o);
    __syncthreads();
    if ((threadIdx.x & 63) == 0) sh[threadIdx.x >> 6] = v;
    __syncthreads();
    return sh[0] + sh[1] + sh[2] + sh[3];
}
__global__ void k_ln(const bf* x, bf* y, int times) {
    __shared__ float sh[4];
    const int r = blockIdx.x, t = threadIdx.x;
    float a = b2f(x[r * 512 + t]), b = b2f(x[r * 512 + 256 + t]);
    for (int i = 0; i < times; ++i) {
        float m = blksum(a + b, sh) / 512.f;
        float v = blksum((a - m) * (a - m) + (b - m) * (b - m), sh) / 512.f;
        float rs = rsqrtf(v + 1e-5f);
        a = rb((a - m) * rs); b = rb((b - m) * rs);
    }
    y[r * 512 + t] = f2b(a); y[r * 512 + 256 + t] = f2b(b);
}
__global__ void k_gemm(const bf* A, int sam, int sak, const bf* B, int ldb, bf* C, int ldc, int M, int K, int mode, const bf* res, const bf* bias) {
    __shared__ float As[16][65], Bs[16][65];
    const int t = threadIdx.x, tx = t & 15, ty = t >> 4, m0 = blockIdx.y * 64, n0 = blockIdx.x * 64;
    float acc[4][4] = {};
    for (int k0 = 0; k0 < K; k0 += 16) {
        for (int i = t; i < 1024; i += 256) {
            int kk, mm; if (sak == 1) { kk = i & 15; mm = i >> 4; } else { mm = i & 63; kk = i >> 6; }
            As[kk][mm] = (m0 + mm < M) ? b2f(A[(size_t)(m0 + mm) * sam + (size_t)(k0 + kk) * sak]) : 0.f;
            int nn = i & 63, k2 = i >> 6;
            Bs[k2][nn] = b2f(B[(size_t)(k0 + k2) * ldb + n0 + nn]);
        }
        __syncthreads();
        for (int kk = 0; kk < 16; ++kk) {
            float a[4], b[4];
            for (int i = 0; i < 4; ++i) { a[i] = As[kk][ty * 4 + i]; b[i] = Bs[kk][tx * 4 + i]; }
            for (int i = 0; i < 4; ++i) for (int j = 0; j < 4; ++j) acc[i][j] += a[i] * b[j];
        }
        __syncthreads();
    }
    for (int i = 0; i < 4; ++i) for (int j = 0; j < 4; ++j) {
        int m = m0 + ty * 4 + i, n = n0 + tx * 4 + j; if (m >= M) continue;
        float v = acc[i][j];
        if (mode == 0) C[(size_t)m * ldc + n] = f2b(v);
        else if (mode == 1) C[(size_t)m * ldc + n] = f2b(rb(v) + b2f(res[(size_t)m * ldc + n]));
        else if (mode == 2) { v = rb(rb(v) + b2f(bias[n])); float u = 0.7978845608f * (v + 0.044715f * v * v * v); C[(size_t)m * ldc + n] = f2b(0.5f * v * (1.f + tanhf(u))); }
        else C[(size_t)n * ldc + m] = f2b(rb(rb(v) + b2f(bias[n])) + b2f(res[(size_t)n * ldc + m]));
    }
}
__global__ void k_attn(const bf* Q, const bf* Km, const bf* Vm, const bf* Kx, const bf* Vx, bf* O, int nq, int ismem, int seg0) {
    __shared__ float P[32][41];
    const int h = blockIdx.x, s = blockIdx.y + seg0, t = threadIdx.x;
    const bf* q = Q + (size_t)s * nq * 4096 + h * 128;
    for (int i = t; i < nq * 40; i += 256) {
        int r = i / 40, c = i % 40;
        const bf* k = c < 8 ? Km + ((size_t)s * 8 + c) * 4096 + h * 128 : Kx + ((size_t)s * 32 + c - 8) * 4096 + h * 128;
        float a = 0; for (int e = 0; e < 128; ++e) a += b2f(q[r * 4096 + e]) * b2f(k[e]);
        a *= 0.08838834764f;
        if (!ismem && c - 8 > r) a = -1e9f;
        P[r][c] = a;
    }
    __syncthreads();
    if (t < nq) { float m = -1e30f; for (int c = 0; c < 40; ++c) m = fmaxf(m, P[t][c]); float l = 0; for (int c = 0; c < 40; ++c) { P[t][c] = __expf(P[t][c] - m); l += P[t][c]; }
        for (int c = 0; c < 40; ++c) P[t][c] = rb(P[t][c] / l); }
    __syncthreads();
    for (int i = t; i < nq * 128; i += 256) {
        int r = i / 128, e = i % 128; float a = 0;
        for (int c = 0; c < 40; ++c) { const bf* v = c < 8 ? Vm + ((size_t)s * 8 + c) * 4096 + h * 128 : Vx + ((size_t)s * 32 + c - 8) * 4096 + h * 128; a += P[r][c] * b2f(v[e]); }
        O[((size_t)s * nq + r) * 4096 + h * 128 + e] = f2b(a);
    }
}
extern "C" void kernel_launch(void* const* d_in, const int* in_sizes, int n_in, void* d_out, int out_size, void* d_ws, size_t ws_size, hipStream_t stream) {
    const bf* x = (const bf*)d_in[0]; const bf* mem0 = (const bf*)d_in[1]; const bf* Wq = (const bf*)d_in[2]; const bf* Wk = (const bf*)d_in[3];
    const bf* Wv = (const bf*)d_in[4]; const bf* Wo = (const bf*)d_in[5]; const bf* w1 = (const bf*)d_in[8]; const bf* b1 = (const bf*)d_in[9];
    const bf* w2 = (const bf*)d_in[10]; const bf* b2 = (const bf*)d_in[11];
    bf* ws = (bf*)d_ws; size_t o = 0;
    auto al = [&](size_t n) { bf* p = ws + o; o += n; return p; };
    bf* xn = al(2048 * 512); bf* Q = al(2048 * 4096); bf* K = al(2048 * 4096); bf* V = al(2048 * 4096); bf* O = al(2048 * 4096);
    bf* mem = al(65 * 8 * 512); bf* Qm = al(64 * 8 * 4096); bf* Km = al(64 * 8 * 4096); bf* Vm = al(64 * 8 * 4096); bf* Om = al(64 * 8 * 4096);
    bf* h = al(2048 * 512); bf* hn = al(2048 * 512); bf* u = al(512 * 1792);
    k_ln<<<2048, 256, 0, stream>>>(x, xn, 1);
    k_gemm<<<dim3(64, 32), 256, 0, stream>>>(xn, 512, 1, Wq, 4096, Q, 4096, 2048, 512, 0, nullptr, nullptr);
    k_gemm<<<dim3(64, 32), 256, 0, stream>>>(xn, 512, 1, Wk, 4096, K, 4096, 2048, 512, 0, nullptr, nullptr);
    k_gemm<<<dim3(64, 32), 256, 0, stream>>>(xn, 512, 1, Wv, 4096, V, 4096, 2048, 512, 0, nullptr, nullptr);
    hipMemcpyAsync(mem, mem0, 8 * 512 * 2, hipMemcpyDeviceToDevice, stream);
    for (int s = 0; s < 64; ++s) {
        const bf* m = mem + s * 4096;
        k_gemm<<<dim3(64, 1), 256, 0, stream>>>(m, 512, 1, Wq, 4096, Qm + (size_t)s * 8 * 4096, 4096, 8, 512, 0, nullptr, nullptr);
        k_gemm<<<dim3(64, 1), 256, 0, stream>>>(m, 512, 1, Wk, 4096, Km + (size_t)s * 8 * 4096, 4096, 8, 512, 0, nullptr, nullptr);
        k_gemm<<<dim3(64, 1), 256, 0, stream>>>(m, 512, 1, Wv, 4096, Vm + (size_t)s * 8 * 4096, 4096, 8, 512, 0, nullptr, nullptr);
        k_attn<<<dim3(32, 1), 256, 0, stream>>>(Qm, Km, Vm, K, V, Om, 8, 1, s);
        k_gemm<<<dim3(8, 1), 256, 0, stream>>>(Om + (size_t)s * 8 * 4096, 4096, 1, Wo, 512, mem + (s + 1) * 4096, 512, 8, 4096, 0, nullptr, nullptr);
    }
    k_attn<<<dim3(32, 64), 256, 0, stream>>>(Q, Km, Vm, K, V, O, 32, 0, 0);
    k_gemm<<<dim3(8, 32), 256, 0, stream>>>(O, 4096, 1, Wo, 512, h, 512, 2048, 4096, 1, x, nullptr);
    k_ln<<<2048, 256, 0, stream>>>(h, hn, 2);
    k_gemm<<<dim3(28, 8), 256, 0, stream>>>(hn, 1, 512, w1, 1792, u, 1792, 512, 2048, 2, nullptr, b1);
    k_gemm<<<dim3(32, 8), 256, 0, stream>>>(u, 1792, 1, w2, 2048, (bf*)d_out, 512, 512, 1792, 3, h, b2);
}
```

```cpp
#include <hip/hip_runtime.h>
#include <hip/hip_bf16.h>
typedef unsigned short bf;
typedef unsigned v4u __attribute__((ext_vector_type(4)));
typedef unsigned v2u __attribute__((ext_vector_type(2)));
typedef float v4f __attribute__((ext_vector_type(4)));
typedef float v2f __attribute__((ext_vector_type(2)));
typedef short v4s __attribute__((ext_vector_type(4)));
typedef __bf16 v8b __attribute__((ext_vector_type(8)));
typedef __bf16 v2b __attribute__((ext_vector_type(2)));
#define LAS __attribute__((address_space(3)))
#define GAS __attribute__((address_space(1)))
#define DI __device__ __forceinline__
#define RLX_AGENT __ATOMIC_RELAXED, __HIP_MEMORY_SCOPE_AGENT
typedef __amdgpu_buffer_rsrc_t rsrc_t;

constexpr size_t WS_COMM = 0;
constexpr size_t WS_CTL = 2u << 20;
constexpr size_t WS_XF = 4u << 20;
constexpr size_t WS_UF = 8u << 20;
constexpr size_t WS_H = 128u << 20;
constexpr size_t WS_HN = WS_H + (4u << 20);
constexpr size_t WS_KV = 12u << 20;
constexpr size_t WS_KM = WS_KV + (64u << 20);
constexpr size_t WS_PT = WS_KM + (32u << 20);
constexpr unsigned BT_R = 832u << 10, BT_A = BT_R + 32768, BT_D = BT_A + 32768, FE_XF = BT_D + 1024, FE_RD = FE_XF + 8192, FE_KV = FE_RD + 8192, FE_INFO = FE_KV + 4096;
constexpr unsigned FL_KM = 272u << 10, FL_QA = FL_KM + 16384, FL_TR = FL_QA + 16384, FL_U = FL_TR + 32768, FL_END = FL_U + 16384;
#define QA_DELAY 3
#define TR_DELAY 0
#define QA_HINT 4
#define G1_HINT 3
#define CAL_PASSES 3
#define WARM_POL ""
#define CODEWARM 0
#define WARM_DELAY 0
#define QAPOLL1 1
#define KVX 0
#define KMX 0
#define W1EARLY 2
#define XFFAR 0
#define G2PF 3
constexpr unsigned SPIN_CAP = 1u << 18;

DI float b2f(bf x) { return __uint_as_float((unsigned)x << 16); }
DI unsigned pk2(float a, float b) { v2b p = __builtin_convertvector((v2f){a, b}, v2b); return __builtin_bit_cast(unsigned, p); }
DI float lo2f(unsigned u) { return __uint_as_float(u << 16); }
DI float hi2f(unsigned u) { return __uint_as_float(u & 0xffff0000u); }
DI float rb(float f) { return lo2f(pk2(f, f)); }
DI v4u mkrsrc(const void* p, unsigned bytes) {
    unsigned long long a = (unsigned long long)p;
    v4u r = {(unsigned)__builtin_amdgcn_readfirstlane((unsigned)a), (unsigned)__builtin_amdgcn_readfirstlane((unsigned)(a >> 32)), bytes, 0x00020000u};
    return r;
}
DI rsrc_t mkr(const void* p, unsigned bytes) { return __builtin_amdgcn_make_buffer_rsrc((void*)p, 0, bytes, 0x00020000); }
DI v4f mfma32(v4u a, v4u b, v4f c) { return __builtin_amdgcn_mfma_f32_16x16x32_bf16(__builtin_bit_cast(v8b, a), __builtin_bit_cast(v8b, b), c, 0, 0, 0); }
DI v4f mfma16(v2u a, v2u b, v4f c) { return __builtin_amdgcn_mfma_f32_16x16x16bf16_1k(__builtin_bit_cast(v4s, a), __builtin_bit_cast(v4s, b), c, 0, 0, 0); }
template <bool WA, bool FIRST> DI void mfa(v4f& c, const v4u& wt, const v4u& x) {
    if (FIRST) { if (WA) asm volatile("v_mfma_f32_16x16x32_bf16 %0, %1, %2, 0" : "=&v"(c) : "a"(wt), "v"(x)); else asm volatile("v_mfma_f32_16x16x32_bf16 %0, %2, %1, 0" : "=&v"(c) : "a"(wt), "v"(x)); }
    else { if (WA) asm volatile("v_mfma_f32_16x16x32_bf16 %0, %1, %2, %0" : "+v"(c) : "a"(wt), "v"(x)); else asm volatile("v_mfma_f32_16x16x32_bf16 %0, %2, %1, %0" : "+v"(c) : "a"(wt), "v"(x)); }
}
template <bool FIRST> DI void mfv(v4f& c, const v4u& a, const v4u& b) {
    if (FIRST) asm volatile("s_nop 1\n\tv_mfma_f32_16x16x32_bf16 %0, %1, %2, 0" : "=&v"(c) : "v"(a), "v"(b));
    else asm volatile("s_nop 1\n\tv_mfma_f32_16x16x32_bf16 %0, %1, %2, %0" : "+v"(c) : "v"(a), "v"(b));
}
#define MFDONE4(a, b, c, d) asm volatile("s_nop 7" : "+v"(a), "+v"(b), "+v"(c), "+v"(d))
#define MFKEEP4(a, b, c, d) asm volatile("" : "+v"(a), "+v"(b), "+v"(c), "+v"(d))
DI v2u trd(unsigned addr) { return __builtin_bit_cast(v2u, __builtin_amdgcn_ds_read_tr16_b64_v4i16((LAS v4s*)(size_t)addr)); }
DI v4u trf(unsigned a0, unsigned a1) { v2u x = trd(a0), y = trd(a1); return (v4u){x.x, x.y, y.x, y.y}; }
DI v4u toacc(v4u v) { asm volatile("" : "+a"(v)); return v; }
DI v4u ldsr(unsigned addr) { return *(LAS v4u*)(size_t)addr; }
DI void ldsw_(unsigned addr, v4u v) { *(LAS v4u*)(size_t)addr = v; }
DI v4u pack8(v4f a, v4f b) { return (v4u){pk2(a[0], a[1]), pk2(a[2], a[3]), pk2(b[0], b[1]), pk2(b[2], b[3])}; }
DI void dma16(v4u rs, unsigned voff, unsigned soff, unsigned ldsdst) {
    asm volatile("s_mov_b32 m0, %3\n\ts_nop 0\n\tbuffer_load_dwordx4 %0, %1, %2 offen lds" :: "v"(voff), "s"(rs), "s"(soff), "s"(ldsdst) : "memory");
}
DI void dma16s(v4u rs, unsigned voff, unsigned soff, unsigned ldsdst) {
    asm volatile("s_mov_b32 m0, %3\n\ts_nop 0\n\tbuffer_load_dwordx4 %0, %1, %2 offen sc1 lds" :: "v"(voff), "s"(rs), "s"(soff), "s"(ldsdst) : "memory");
}
DI void st16(v4u v, v4u rs, unsigned voff, unsigned soff) {
    asm volatile("buffer_store_dwordx4 %0, %1, %2, %3 offen sc1\n\ts_nop 1" :: "v"(v), "v"(voff), "s"(rs), "s"(soff) : "memory");
}
#define VMWAIT(n) asm volatile("s_waitcnt vmcnt(" #n ")" ::: "memory")
#define BAR() do { asm volatile("s_waitcnt lgkmcnt(0)" ::: "memory"); __builtin_amdgcn_s_barrier(); asm volatile("" ::: "memory"); } while (0)
DI int lane_id() { int l; asm volatile("v_mbcnt_lo_u32_b32 %0, -1, 0\n\tv_mbcnt_hi_u32_b32 %0, -1, %0" : "=v"(l)); return l; }
DI int xslot(int lane) { return 32 * (lane >> 5) + 8 * (lane & 3) + 4 * ((lane >> 4) & 1) + ((lane >> 2) & 3); }
DI unsigned uni(unsigned x) { return __builtin_amdgcn_readfirstlane(x); }
DI bool wait_ge(const char* p, unsigned want) {
#pragma nounroll
    for (unsigned i = 0; i < SPIN_CAP; ++i) {
        if (uni(__hip_atomic_load((GAS unsigned*)p, RLX_AGENT)) >= want) { asm volatile("" ::: "memory"); return true; }
        __builtin_amdgcn_s_sleep(2);
    }
    return false;
}
DI unsigned poll_ge(const char* p, unsigned want) {
    unsigned v = 0;
#pragma nounroll
    for (unsigned i = 0; i < SPIN_CAP; ++i) {
        v = uni(__hip_atomic_load((GAS unsigned*)p, RLX_AGENT));
        if (v >= want) break;
        __builtin_amdgcn_s_sleep(2);
    }
    asm volatile("" ::: "memory");
    return v;
}
DI unsigned ld_flags(const char* p, int lane) { return __hip_atomic_load((GAS unsigned*)(p + (lane & 31) * 128), RLX_AGENT); }
DI unsigned ld_async(const char* a) {
    unsigned v; asm volatile("global_load_dword %0, %1, off sc1" : "=v"(v) : "v"(a) : "memory"); return v;
}
DI bool wait_all_ge(const char* p, int lane, unsigned want) {
#pragma nounroll
    for (unsigned i = 0; i < SPIN_CAP; ++i) {
        if (__all(ld_flags(p, lane) >= want)) { asm volatile("" ::: "memory"); return true; }
        __builtin_amdgcn_s_sleep(2);
    }
    return false;
}
DI unsigned pt_slot(int s, unsigned fb) { const int k = s == 63 ? 8 : (s & 7); return k * (2u << 20) + (fb ^ ((k & 1) * 8192)); }
DI unsigned hrow(int row, unsigned fb) { return (row >> 3) * 16384 + (row & 7) * 1024 + (fb ^ (((row >> 10) & 1) * 8192)); }
DI unsigned xf_off(int s, int w, unsigned fb) { return (s * 4 + w) * 16384 + (XFFAR ? (fb ^ (((s >> 5) & 1) * 8192)) : (w & 1) * 8192); }
DI unsigned umax4(v4u v) { return max(max(v.x, v.y), max(v.z, v.w)); }
DI unsigned tr_flags(int s) { return FL_TR + ((s >> 1) & 1) * 16384 + (s & 1) * 4096; }
DI void pause_us(int us) { for (int i = 0; i < us; ++i) __builtin_amdgcn_s_sleep(36); }
DI float xmax_g(float v) {
    auto r = __builtin_amdgcn_permlane32_swap(__float_as_uint(v), __float_as_uint(v), false, false);
    v = fmaxf(__uint_as_float(r[0]), __uint_as_float(r[1]));
    r = __builtin_amdgcn_permlane16_swap(__float_as_uint(v), __float_as_uint(v), false, false);
    return fmaxf(__uint_as_float(r[0]), __uint_as_float(r[1]));
}
DI float xsum_g(float v) {
    auto r = __builtin_amdgcn_permlane32_swap(__float_as_uint(v), __float_as_uint(v), false, false);
    v = __uint_as_float(r[0]) + __uint_as_float(r[1]);
    r = __builtin_amdgcn_permlane16_swap(__float_as_uint(v), __float_as_uint(v), false, false);
    return __uint_as_float(r[0]) + __uint_as_float(r[1]);
}
template <int C> DI float dpp_add(float v) { return v + __builtin_bit_cast(float, __builtin_amdgcn_update_dpp(0, __builtin_bit_cast(int, v), C, 0xf, 0xf, false)); }
DI float wave_sum(float v) {
    v = dpp_add<0x128>(v); v = dpp_add<0x124>(v); v = dpp_add<0x122>(v); v = dpp_add<0x121>(v);
    return xsum_g(v);
}
DI void ln8(float (&v)[8]) {
    float s = 0;
#pragma unroll
    for (int i = 0; i < 8; ++i) s += v[i];
    const float m = wave_sum(s) * (1.f / 512.f);
    float q = 0;
#pragma unroll
    for (int i = 0; i < 8; ++i) { v[i] -= m; q += v[i] * v[i]; }
    const float rs = rsqrtf(wave_sum(q) * (1.f / 512.f) + 1e-5f);
#pragma unroll
    for (int i = 0; i < 8; i += 2) { const unsigned p = pk2(v[i] * rs, v[i + 1] * rs); v[i] = lo2f(p); v[i + 1] = hi2f(p); }
}
DI void unpack8(v4u u, float (&v)[8]) {
#pragma unroll
    for (int i = 0; i < 4; ++i) { v[2 * i] = lo2f(u[i]); v[2 * i + 1] = hi2f(u[i]); }
}
DI v4u repack8(const float (&v)[8]) { return (v4u){pk2(v[0], v[1]), pk2(v[2], v[3]), pk2(v[4], v[5]), pk2(v[6], v[7])}; }

#define TS(p, k) do {} while (0)
DI void head_loop(unsigned ldsw) {
    asm volatile(
        "s_nop 4\n\t"
        "v_mbcnt_lo_u32_b32 v16, -1, 0\n\t"
        "v_mbcnt_hi_u32_b32 v16, -1, v16\n\t"
        "v_lshlrev_b32 v17, 4, v16\n\t"
        "v_add_u32 v16, %[ldsw], v17\n\t"
        "ds_read_b128 v[0:3], v16\n\t"
        "ds_read_b128 v[4:7], v16 offset:1024\n\t"
        "ds_read_b128 v[8:11], v16 offset:2048\n\t"
        "ds_read_b128 v[48:51], v16 offset:3072\n\t"
        "v_lshrrev_b32 v17, 2, v17\n\t"
        "v_add_u32 v17, %[ldsw], v17\n\t"
        "ds_read_b32 v52, v17 offset:4096\n\t"
        "s_mov_b32 s50, 0x05040100\n\t"
        "s_mov_b32 s51, 0x07060302\n\t"
        "s_mov_b32 s35, %[ldsw]\n\t"
        "s_waitcnt lgkmcnt(0)\n\t"
        "v_readlane_b32 s8, v52, 0\n\t"
        "v_readlane_b32 s9, v52, 1\n\t"
        "v_readlane_b32 s10, v52, 2\n\t"
        "s_mov_b32 s11, 0x20000\n\t"
        "v_readlane_b32 s12, v52, 3\n\t"
        "v_readlane_b32 s13, v52, 4\n\t"
        "v_readlane_b32 s14, v52, 5\n\t"
        "s_mov_b32 s15, 0x20000\n\t"
        "v_readlane_b32 s16, v52, 6\n\t"
        "v_readlane_b32 s17, v52, 7\n\t"
        "v_readlane_b32 s18, v52, 8\n\t"
        "s_mov_b32 s19, 0x20000\n\t"
        "v_readlane_b32 s64, v52, 9\n\t"
        "v_readlane_b32 s65, v52, 10\n\t"
        "v_readlane_b32 s66, v52, 11\n\t"
        "s_mov_b32 s67, 0x20000\n\t"
        "v_readlane_b32 s68, v52, 12\n\t"
        "v_readlane_b32 s69, v52, 13\n\t"
        "v_readlane_b32 s70, v52, 14\n\t"
        "s_mov_b32 s71, 0x20000\n\t"
        "v_readlane_b32 s72, v52, 15\n\t"
        "v_readlane_b32 s73, v52, 16\n\t"
        "v_readlane_b32 s74, v52, 17\n\t"
        "s_mov_b32 s75, 0x20000\n\t"
        "v_readlane_b32 s76, v52, 18\n\t"
        "v_readlane_b32 s77, v52, 19\n\t"
        "v_readlane_b32 s78, v52, 20\n\t"
        "s_mov_b32 s79, 0x20000\n\t"
        "v_readlane_b32 s20, v52, 21\n\t"
        "v_readlane_b32 s21, v52, 22\n\t"
        "v_readlane_b32 s22, v52, 23\n\t"
        "v_readlane_b32 s23, v52, 24\n\t"
        "v_readlane_b32 s24, v52, 25\n\t"
        "v_readlane_b32 s25, v52, 26\n\t"
        "v_readlane_b32 s26, v52, 27\n\t"
        "v_readlane_b32 s27, v52, 28\n\t"
        "v_readlane_b32 s28, v52, 29\n\t"
        "v_readlane_b32 s29, v52, 30\n\t"
        "v_readlane_b32 s30, v52, 31\n\t"
        "v_readlane_b32 s31, v52, 32\n\t"
        "v_readlane_b32 s34, v52, 33\n\t"
        "v_readlane_b32 s5, v52, 34\n\t"
        "v_readlane_b32 s6, v52, 35\n\t"
        "v_readlane_b32 s7, v52, 36\n\t"
        "v_readlane_b32 s4, v52, 37\n\t"
        "s_nop 4\n\t"
        "s_add_u32 s36, s25, 0\n\t"
        "s_and_b32 s36, s36, 7\n\t"
        "s_lshl_b32 s36, s36, 19\n\t"
        "s_add_u32 s36, s36, s23\n\t"
        "s_add_u32 s37, s25, 2\n\t"
        "s_and_b32 s37, s37, 7\n\t"
        "s_lshl_b32 s37, s37, 19\n\t"
        "s_add_u32 s37, s37, s23\n\t"
        "s_add_u32 s38, s25, 4\n\t"
        "s_and_b32 s38, s38, 7\n\t"
        "s_lshl_b32 s38, s38, 19\n\t"
        "s_add_u32 s38, s38, s23\n\t"
        "s_add_u32 s39, s25, 6\n\t"
        "s_and_b32 s39, s39, 7\n\t"
        "s_lshl_b32 s39, s39, 19\n\t"
        "s_add_u32 s39, s39, s23\n\t"
        "s_add_u32 s40, s25, 0\n\t"
        "s_and_b32 s40, s40, 7\n\t"
        "s_lshl_b32 s40, s40, 19\n\t"
        "s_add_u32 s40, s40, s23\n\t"
        "s_add_u32 s40, s40, 32768\n\t"
        "s_add_u32 s41, s25, 2\n\t"
        "s_and_b32 s41, s41, 7\n\t"
        "s_lshl_b32 s41, s41, 19\n\t"
        "s_add_u32 s41, s41, s23\n\t"
        "s_add_u32 s41, s41, 32768\n\t"
        "s_add_u32 s42, s25, 4\n\t"
        "s_and_b32 s42, s42, 7\n\t"
        "s_lshl_b32 s42, s42, 19\n\t"
        "s_add_u32 s42, s42, s23\n\t"
        "s_add_u32 s42, s42, 32768\n\t"
        "s_add_u32 s43, s25, 6\n\t"
        "s_and_b32 s43, s43, 7\n\t"
        "s_lshl_b32 s43, s43, 19\n\t"
        "s_add_u32 s43, s43, s23\n\t"
        "s_add_u32 s43, s43, 32768\n\t"
        "v_add_u32 v12, 0x3fc00, v10\n\t"
        "v_add_u32 v13, 0x7f800, v10\n\t"
        "v_add_u32 v14, 0xbf400, v10\n\t"
        "v_mov_b32 v15, 0\n\t"
        "buffer_load_dword v59, v15, s[8:11], s34 offen sc1\n\t"
        "s_add_u32 s46, s24, 65536\n\t"
        "buffer_load_dwordx4 v[16:19], v11, s[76:79], s46 offen offset:0 nt\n\t"
        "buffer_load_dwordx4 v[20:23], v11, s[76:79], s46 offen offset:1024 nt\n\t"
        "s_add_u32 s47, s24, 69632\n\t"
        "buffer_load_dwordx4 v[24:27], v11, s[76:79], s47 offen offset:0 nt\n\t"
        "buffer_load_dwordx4 v[28:31], v11, s[76:79], s47 offen offset:1024 nt\n\t"
        "buffer_load_dwordx4 v[32:35], v11, s[76:79], s46 offen offset:2048 nt\n\t"
        "buffer_load_dwordx4 v[36:39], v11, s[76:79], s46 offen offset:3072 nt\n\t"
        "buffer_load_dwordx4 v[40:43], v11, s[76:79], s47 offen offset:2048 nt\n\t"
        "buffer_load_dwordx4 v[44:47], v11, s[76:79], s47 offen offset:3072 nt\n\t"
        "s_add_u32 s46, s24, 0\n\t"
        "buffer_load_dwordx4 v[128:131], v11, s[76:79], s46 offen offset:0 nt\n\t"
        "buffer_load_dwordx4 v[132:135], v11, s[76:79], s46 offen offset:1024 nt\n\t"
        "s_add_u32 s47, s24, 4096\n\t"
        "buffer_load_dwordx4 v[136:139], v11, s[76:79], s47 offen offset:0 nt\n\t"
        "buffer_load_dwordx4 v[140:143], v11, s[76:79], s47 offen offset:1024 nt\n\t"
        "buffer_load_dwordx4 v[144:147], v11, s[76:79], s46 offen offset:2048 nt\n\t"
        "buffer_load_dwordx4 v[148:151], v11, s[76:79], s46 offen offset:3072 nt\n\t"
        "buffer_load_dwordx4 v[152:155], v11, s[76:79], s47 offen offset:2048 nt\n\t"
        "buffer_load_dwordx4 v[156:159], v11, s[76:79], s47 offen offset:3072 nt\n\t"
        "s_add_u32 s46, s24, 32768\n\t"
        "buffer_load_dwordx4 v[160:163], v11, s[76:79], s46 offen offset:0 nt\n\t"
        "buffer_load_dwordx4 v[164:167], v11, s[76:79], s46 offen offset:1024 nt\n\t"
        "s_add_u32 s47, s24, 36864\n\t"
        "buffer_load_dwordx4 v[168:171], v11, s[76:79], s47 offen offset:0 nt\n\t"
        "buffer_load_dwordx4 v[172:175], v11, s[76:79], s47 offen offset:1024 nt\n\t"
        "buffer_load_dwordx4 v[176:179], v11, s[76:79], s46 offen offset:2048 nt\n\t"
        "buffer_load_dwordx4 v[180:183], v11, s[76:79], s46 offen offset:3072 nt\n\t"
        "buffer_load_dwordx4 v[184:187], v11, s[76:79], s47 offen offset:2048 nt\n\t"
        "buffer_load_dwordx4 v[188:191], v11, s[76:79], s47 offen offset:3072 nt\n\t"
        "s_waitcnt vmcnt(24)\n\t"
        "v_add_u32 v59, 1, v59\n\t"
        "s_sub_u32 s53, s34, 32768\n\t"
        "v_readfirstlane_b32 s63, v59\n\t"
        "buffer_store_dword v59, v15, s[8:11], s53 offen sc1\n\t"
        "s_waitcnt vmcnt(17)\n\t"
        "v_perm_b32 v72, v20, v16, s50\n\t"
        "v_perm_b32 v73, v28, v24, s50\n\t"
        "v_perm_b32 v74, v36, v32, s50\n\t"
        "v_perm_b32 v75, v44, v40, s50\n\t"
        "v_perm_b32 v88, v20, v16, s51\n\t"
        "v_perm_b32 v89, v28, v24, s51\n\t"
        "v_perm_b32 v90, v36, v32, s51\n\t"
        "v_perm_b32 v91, v44, v40, s51\n\t"
        "v_perm_b32 v104, v21, v17, s50\n\t"
        "v_perm_b32 v105, v29, v25, s50\n\t"
        "v_perm_b32 v106, v37, v33, s50\n\t"
        "v_perm_b32 v107, v45, v41, s50\n\t"
        "v_perm_b32 v120, v21, v17, s51\n\t"
        "v_perm_b32 v121, v29, v25, s51\n\t"
        "v_perm_b32 v122, v37, v33, s51\n\t"
        "v_perm_b32 v123, v45, v41, s51\n\t"
        "v_perm_b32 v200, v22, v18, s50\n\t"
        "v_perm_b32 v201, v30, v26, s50\n\t"
        "v_perm_b32 v202, v38, v34, s50\n\t"
        "v_perm_b32 v203, v46, v42, s50\n\t"
        "v_perm_b32 v216, v22, v18, s51\n\t"
        "v_perm_b32 v217, v30, v26, s51\n\t"
        "v_perm_b32 v218, v38, v34, s51\n\t"
        "v_perm_b32 v219, v46, v42, s51\n\t"
        "v_perm_b32 v232, v23, v19, s50\n\t"
        "v_perm_b32 v233, v31, v27, s50\n\t"
        "v_perm_b32 v234, v39, v35, s50\n\t"
        "v_perm_b32 v235, v47, v43, s50\n\t"
        "v_perm_b32 v248, v23, v19, s51\n\t"
        "v_perm_b32 v249, v31, v27, s51\n\t"
        "v_perm_b32 v250, v39, v35, s51\n\t"
        "v_perm_b32 v251, v47, v43, s51\n\t"
        "s_add_u32 s46, s24, 98304\n\t"
        "buffer_load_dwordx4 v[16:19], v11, s[76:79], s46 offen offset:0 nt\n\t"
        "buffer_load_dwordx4 v[20:23], v11, s[76:79], s46 offen offset:1024 nt\n\t"
        "s_add_u32 s47, s24, 102400\n\t"
        "buffer_load_dwordx4 v[24:27], v11, s[76:79], s47 offen offset:0 nt\n\t"
        "buffer_load_dwordx4 v[28:31], v11, s[76:79], s47 offen offset:1024 nt\n\t"
        "buffer_load_dwordx4 v[32:35], v11, s[76:79], s46 offen offset:2048 nt\n\t"
        "buffer_load_dwordx4 v[36:39], v11, s[76:79], s46 offen offset:3072 nt\n\t"
        "buffer_load_dwordx4 v[40:43], v11, s[76:79], s47 offen offset:2048 nt\n\t"
        "buffer_load_dwordx4 v[44:47], v11, s[76:79], s47 offen offset:3072 nt\n\t"
        "s_add_u32 m0, s35, 0\n\t"
        "s_nop 0\n\t"
        "buffer_load_dwordx4 v10, s[68:71], s36 offen offset:0 lds\n\t"
        "buffer_load_dwordx4 v12, s[68:71], s36 offen offset:1024 lds\n\t"
        "buffer_load_dwordx4 v13, s[68:71], s36 offen offset:2048 lds\n\t"
        "buffer_load_dwordx4 v14, s[68:71], s36 offen offset:3072 lds\n\t"
        "s_add_u32 m0, s35, 4096\n\t"
        "s_nop 0\n\t"
        "buffer_load_dwordx4 v10, s[68:71], s37 offen offset:0 lds\n\t"
        "buffer_load_dwordx4 v12, s[68:71], s37 offen offset:1024 lds\n\t"
        "buffer_load_dwordx4 v13, s[68:71], s37 offen offset:2048 lds\n\t"
        "buffer_load_dwordx4 v14, s[68:71], s37 offen offset:3072 lds\n\t"
        "s_add_u32 m0, s35, 8192\n\t"
        "s_nop 0\n\t"
        "buffer_load_dwordx4 v10, s[68:71], s38 offen offset:0 lds\n\t"
        "buffer_load_dwordx4 v12, s[68:71], s38 offen offset:1024 lds\n\t"
        "buffer_load_dwordx4 v13, s[68:71], s38 offen offset:2048 lds\n\t"
        "buffer_load_dwordx4 v14, s[68:71], s38 offen offset:3072 lds\n\t"
        "s_add_u32 m0, s35, 12288\n\t"
        "s_nop 0\n\t"
        "buffer_load_dwordx4 v10, s[68:71], s41 offen offset:0 lds\n\t"
        "buffer_load_dwordx4 v12, s[68:71], s41 offen offset:1024 lds\n\t"
        "buffer_load_dwordx4 v13, s[68:71], s41 offen offset:2048 lds\n\t"
        "buffer_load_dwordx4 v14, s[68:71], s41 offen offset:3072 lds\n\t"
        "s_add_u32 m0, s35, 16384\n\t"
        "s_nop 0\n\t"
        "buffer_load_dwordx4 v10, s[68:71], s42 offen offset:0 lds\n\t"
        "buffer_load_dwordx4 v12, s[68:71], s42 offen offset:1024 lds\n\t"
        "buffer_load_dwordx4 v13, s[68:71], s42 offen offset:2048 lds\n\t"
        "buffer_load_dwordx4 v14, s[68:71], s42 offen offset:3072 lds\n\t"
        "s_add_u32 m0, s35, 20480\n\t"
        "s_nop 0\n\t"
        "buffer_load_dwordx4 v10, s[68:71], s43 offen offset:0 lds\n\t"
        "buffer_load_dwordx4 v12, s[68:71], s43 offen offset:1024 lds\n\t"
        "buffer_load_dwordx4 v13, s[68:71], s43 offen offset:2048 lds\n\t"
        "buffer_load_dwordx4 v14, s[68:71], s43 offen offset:3072 lds\n\t"
        "s_add_u32 m0, s35, 24576\n\t"
        "s_nop 0\n\t"
        "buffer_load_dwordx4 v10, s[72:75], s37 offen offset:0 lds\n\t"
        "buffer_load_dwordx4 v12, s[72:75], s37 offen offset:1024 lds\n\t"
        "buffer_load_dwordx4 v13, s[72:75], s37 offen offset:2048 lds\n\t"
        "buffer_load_dwordx4 v14, s[72:75], s37 offen offset:3072 lds\n\t"
        "s_add_u32 m0, s35, 28672\n\t"
        "s_nop 0\n\t"
        "buffer_load_dwordx4 v10, s[72:75], s38 offen offset:0 lds\n\t"
        "buffer_load_dwordx4 v12, s[72:75], s38 offen offset:1024 lds\n\t"
        "buffer_load_dwordx4 v13, s[72:75], s38 offen offset:2048 lds\n\t"
        "buffer_load_dwordx4 v14, s[72:75], s38 offen offset:3072 lds\n\t"
        "s_waitcnt vmcnt(49)\n\t"
        "v_perm_b32 v64, v132, v128, s50\n\t"
        "v_perm_b32 v65, v140, v136, s50\n\t"
        "v_perm_b32 v66, v148, v144, s50\n\t"
        "v_perm_b32 v67, v156, v152, s50\n\t"
        "v_perm_b32 v80, v132, v128, s51\n\t"
        "v_perm_b32 v81, v140, v136, s51\n\t"
        "v_perm_b32 v82, v148, v144, s51\n\t"
        "v_perm_b32 v83, v156, v152, s51\n\t"
        "v_perm_b32 v96, v133, v129, s50\n\t"
        "v_perm_b32 v97, v141, v137, s50\n\t"
        "v_perm_b32 v98, v149, v145, s50\n\t"
        "v_perm_b32 v99, v157, v153, s50\n\t"
        "v_perm_b32 v112, v133, v129, s51\n\t"
        "v_perm_b32 v113, v141, v137, s51\n\t"
        "v_perm_b32 v114, v149, v145, s51\n\t"
        "v_perm_b32 v115, v157, v153, s51\n\t"
        "v_perm_b32 v192, v134, v130, s50\n\t"
        "v_perm_b32 v193, v142, v138, s50\n\t"
        "v_perm_b32 v194, v150, v146, s50\n\t"
        "v_perm_b32 v195, v158, v154, s50\n\t"
        "v_perm_b32 v208, v134, v130, s51\n\t"
        "v_perm_b32 v209, v142, v138, s51\n\t"
        "v_perm_b32 v210, v150, v146, s51\n\t"
        "v_perm_b32 v211, v158, v154, s51\n\t"
        "v_perm_b32 v224, v135, v131, s50\n\t"
        "v_perm_b32 v225, v143, v139, s50\n\t"
        "v_perm_b32 v226, v151, v147, s50\n\t"
        "v_perm_b32 v227, v159, v155, s50\n\t"
        "v_perm_b32 v240, v135, v131, s51\n\t"
        "v_perm_b32 v241, v143, v139, s51\n\t"
        "v_perm_b32 v242, v151, v147, s51\n\t"
        "v_perm_b32 v243, v159, v155, s51\n\t"
        "s_waitcnt vmcnt(41)\n\t"
        "v_perm_b32 v68, v164, v160, s50\n\t"
        "v_perm_b32 v69, v172, v168, s50\n\t"
        "v_perm_b32 v70, v180, v176, s50\n\t"
        "v_perm_b32 v71, v188, v184, s50\n\t"
        "v_perm_b32 v84, v164, v160, s51\n\t"
        "v_perm_b32 v85, v172, v168, s51\n\t"
        "v_perm_b32 v86, v180, v176, s51\n\t"
        "v_perm_b32 v87, v188, v184, s51\n\t"
        "v_perm_b32 v100, v165, v161, s50\n\t"
        "v_perm_b32 v101, v173, v169, s50\n\t"
        "v_perm_b32 v102, v181, v177, s50\n\t"
        "v_perm_b32 v103, v189, v185, s50\n\t"
        "v_perm_b32 v116, v165, v161, s51\n\t"
        "v_perm_b32 v117, v173, v169, s51\n\t"
        "v_perm_b32 v118, v181, v177, s51\n\t"
        "v_perm_b32 v119, v189, v185, s51\n\t"
        "v_perm_b32 v196, v166, v162, s50\n\t"
        "v_perm_b32 v197, v174, v170, s50\n\t"
        "v_perm_b32 v198, v182, v178, s50\n\t"
        "v_perm_b32 v199, v190, v186, s50\n\t"
        "v_perm_b32 v212, v166, v162, s51\n\t"
        "v_perm_b32 v213, v174, v170, s51\n\t"
        "v_perm_b32 v214, v182, v178, s51\n\t"
        "v_perm_b32 v215, v190, v186, s51\n\t"
        "v_perm_b32 v228, v167, v163, s50\n\t"
        "v_perm_b32 v229, v175, v171, s50\n\t"
        "v_perm_b32 v230, v183, v179, s50\n\t"
        "v_perm_b32 v231, v191, v187, s50\n\t"
        "v_perm_b32 v244, v167, v163, s51\n\t"
        "v_perm_b32 v245, v175, v171, s51\n\t"
        "v_perm_b32 v246, v183, v179, s51\n\t"
        "v_perm_b32 v247, v191, v187, s51\n\t"
        "s_waitcnt vmcnt(32)\n\t"
        "v_perm_b32 v76, v20, v16, s50\n\t"
        "v_perm_b32 v77, v28, v24, s50\n\t"
        "v_perm_b32 v78, v36, v32, s50\n\t"
        "v_perm_b32 v79, v44, v40, s50\n\t"
        "v_perm_b32 v92, v20, v16, s51\n\t"
        "v_perm_b32 v93, v28, v24, s51\n\t"
        "v_perm_b32 v94, v36, v32, s51\n\t"
        "v_perm_b32 v95, v44, v40, s51\n\t"
        "v_perm_b32 v108, v21, v17, s50\n\t"
        "v_perm_b32 v109, v29, v25, s50\n\t"
        "v_perm_b32 v110, v37, v33, s50\n\t"
        "v_perm_b32 v111, v45, v41, s50\n\t"
        "v_perm_b32 v124, v21, v17, s51\n\t"
        "v_perm_b32 v125, v29, v25, s51\n\t"
        "v_perm_b32 v126, v37, v33, s51\n\t"
        "v_perm_b32 v127, v45, v41, s51\n\t"
        "v_perm_b32 v204, v22, v18, s50\n\t"
        "v_perm_b32 v205, v30, v26, s50\n\t"
        "v_perm_b32 v206, v38, v34, s50\n\t"
        "v_perm_b32 v207, v46, v42, s50\n\t"
        "v_perm_b32 v220, v22, v18, s51\n\t"
        "v_perm_b32 v221, v30, v26, s51\n\t"
        "v_perm_b32 v222, v38, v34, s51\n\t"
        "v_perm_b32 v223, v46, v42, s51\n\t"
        "v_perm_b32 v236, v23, v19, s50\n\t"
        "v_perm_b32 v237, v31, v27, s50\n\t"
        "v_perm_b32 v238, v39, v35, s50\n\t"
        "v_perm_b32 v239, v47, v43, s50\n\t"
        "v_perm_b32 v252, v23, v19, s51\n\t"
        "v_perm_b32 v253, v31, v27, s51\n\t"
        "v_perm_b32 v254, v39, v35, s51\n\t"
        "v_perm_b32 v255, v47, v43, s51\n\t"
        "s_waitcnt vmcnt(28)\n\t"
        "ds_read_b64_tr_b16 a[0:1], v48 offset:0\n\t"
        "ds_read_b64_tr_b16 a[2:3], v49 offset:0\n\t"
        "ds_read_b64_tr_b16 a[4:5], v48 offset:2048\n\t"
        "ds_read_b64_tr_b16 a[6:7], v49 offset:2048\n\t"
        "ds_read_b64_tr_b16 a[8:9], v50 offset:0\n\t"
        "ds_read_b64_tr_b16 a[10:11], v51 offset:0\n\t"
        "ds_read_b64_tr_b16 a[12:13], v50 offset:2048\n\t"
        "ds_read_b64_tr_b16 a[14:15], v51 offset:2048\n\t"
        "s_waitcnt vmcnt(24)\n\t"
        "ds_read_b64_tr_b16 a[16:17], v48 offset:4096\n\t"
        "ds_read_b64_tr_b16 a[18:19], v49 offset:4096\n\t"
        "ds_read_b64_tr_b16 a[20:21], v48 offset:6144\n\t"
        "ds_read_b64_tr_b16 a[22:23], v49 offset:6144\n\t"
        "ds_read_b64_tr_b16 a[24:25], v50 offset:4096\n\t"
        "ds_read_b64_tr_b16 a[26:27], v51 offset:4096\n\t"
        "ds_read_b64_tr_b16 a[28:29], v50 offset:6144\n\t"
        "ds_read_b64_tr_b16 a[30:31], v51 offset:6144\n\t"
        "s_waitcnt lgkmcnt(8)\n\t"
        "s_add_u32 m0, s35, 0\n\t"
        "s_nop 0\n\t"
        "buffer_load_dwordx4 v10, s[72:75], s40 offen offset:0 lds\n\t"
        "buffer_load_dwordx4 v12, s[72:75], s40 offen offset:1024 lds\n\t"
        "buffer_load_dwordx4 v13, s[72:75], s40 offen offset:2048 lds\n\t"
        "buffer_load_dwordx4 v14, s[72:75], s40 offen offset:3072 lds\n\t"
        "s_waitcnt vmcnt(24)\n\t"
        "ds_read_b64_tr_b16 a[32:33], v48 offset:8192\n\t"
        "ds_read_b64_tr_b16 a[34:35], v49 offset:8192\n\t"
        "ds_read_b64_tr_b16 a[36:37], v48 offset:10240\n\t"
        "ds_read_b64_tr_b16 a[38:39], v49 offset:10240\n\t"
        "ds_read_b64_tr_b16 a[40:41], v50 offset:8192\n\t"
        "ds_read_b64_tr_b16 a[42:43], v51 offset:8192\n\t"
        "ds_read_b64_tr_b16 a[44:45], v50 offset:10240\n\t"
        "ds_read_b64_tr_b16 a[46:47], v51 offset:10240\n\t"
        "s_waitcnt lgkmcnt(8)\n\t"
        "s_add_u32 m0, s35, 4096\n\t"
        "s_nop 0\n\t"
        "buffer_load_dwordx4 v10, s[72:75], s41 offen offset:0 lds\n\t"
        "buffer_load_dwordx4 v12, s[72:75], s41 offen offset:1024 lds\n\t"
        "buffer_load_dwordx4 v13, s[72:75], s41 offen offset:2048 lds\n\t"
        "buffer_load_dwordx4 v14, s[72:75], s41 offen offset:3072 lds\n\t"
        "s_waitcnt vmcnt(24)\n\t"
        "ds_read_b64_tr_b16 a[48:49], v48 offset:12288\n\t"
        "ds_read_b64_tr_b16 a[50:51], v49 offset:12288\n\t"
        "ds_read_b64_tr_b16 a[52:53], v48 offset:14336\n\t"
        "ds_read_b64_tr_b16 a[54:55], v49 offset:14336\n\t"
        "ds_read_b64_tr_b16 a[56:57], v50 offset:12288\n\t"
        "ds_read_b64_tr_b16 a[58:59], v51 offset:12288\n\t"
        "ds_read_b64_tr_b16 a[60:61], v50 offset:14336\n\t"
        "ds_read_b64_tr_b16 a[62:63], v51 offset:14336\n\t"
        "s_waitcnt lgkmcnt(8)\n\t"
        "s_add_u32 m0, s35, 8192\n\t"
        "s_nop 0\n\t"
        "buffer_load_dwordx4 v10, s[72:75], s43 offen offset:0 lds\n\t"
        "buffer_load_dwordx4 v12, s[72:75], s43 offen offset:1024 lds\n\t"
        "buffer_load_dwordx4 v13, s[72:75], s43 offen offset:2048 lds\n\t"
        "buffer_load_dwordx4 v14, s[72:75], s43 offen offset:3072 lds\n\t"
        "s_waitcnt vmcnt(24)\n\t"
        "ds_read_b64_tr_b16 a[64:65], v48 offset:16384\n\t"
        "ds_read_b64_tr_b16 a[66:67], v49 offset:16384\n\t"
        "ds_read_b64_tr_b16 a[68:69], v48 offset:18432\n\t"
        "ds_read_b64_tr_b16 a[70:71], v49 offset:18432\n\t"
        "ds_read_b64_tr_b16 a[72:73], v50 offset:16384\n\t"
        "ds_read_b64_tr_b16 a[74:75], v51 offset:16384\n\t"
        "ds_read_b64_tr_b16 a[76:77], v50 offset:18432\n\t"
        "ds_read_b64_tr_b16 a[78:79], v51 offset:18432\n\t"
        "s_waitcnt lgkmcnt(8)\n\t"
        "s_add_u32 m0, s35, 12288\n\t"
        "s_nop 0\n\t"
        "buffer_load_dwordx4 v10, s[64:67], s36 offen offset:0 lds\n\t"
        "buffer_load_dwordx4 v12, s[64:67], s36 offen offset:1024 lds\n\t"
        "buffer_load_dwordx4 v13, s[64:67], s36 offen offset:2048 lds\n\t"
        "buffer_load_dwordx4 v14, s[64:67], s36 offen offset:3072 lds\n\t"
        "s_waitcnt vmcnt(24)\n\t"
        "ds_read_b64_tr_b16 a[80:81], v48 offset:20480\n\t"
        "ds_read_b64_tr_b16 a[82:83], v49 offset:20480\n\t"
        "ds_read_b64_tr_b16 a[84:85], v48 offset:22528\n\t"
        "ds_read_b64_tr_b16 a[86:87], v49 offset:22528\n\t"
        "ds_read_b64_tr_b16 a[88:89], v50 offset:20480\n\t"
        "ds_read_b64_tr_b16 a[90:91], v51 offset:20480\n\t"
        "ds_read_b64_tr_b16 a[92:93], v50 offset:22528\n\t"
        "ds_read_b64_tr_b16 a[94:95], v51 offset:22528\n\t"
        "s_waitcnt lgkmcnt(8)\n\t"
        "s_add_u32 m0, s35, 16384\n\t"
        "s_nop 0\n\t"
        "buffer_load_dwordx4 v10, s[64:67], s39 offen offset:0 lds\n\t"
        "buffer_load_dwordx4 v12, s[64:67], s39 offen offset:1024 lds\n\t"
        "buffer_load_dwordx4 v13, s[64:67], s39 offen offset:2048 lds\n\t"
        "buffer_load_dwordx4 v14, s[64:67], s39 offen offset:3072 lds\n\t"
        "s_waitcnt vmcnt(24)\n\t"
        "ds_read_b64_tr_b16 a[96:97], v48 offset:24576\n\t"
        "ds_read_b64_tr_b16 a[98:99], v49 offset:24576\n\t"
        "ds_read_b64_tr_b16 a[100:101], v48 offset:26624\n\t"
        "ds_read_b64_tr_b16 a[102:103], v49 offset:26624\n\t"
        "ds_read_b64_tr_b16 a[104:105], v50 offset:24576\n\t"
        "ds_read_b64_tr_b16 a[106:107], v51 offset:24576\n\t"
        "ds_read_b64_tr_b16 a[108:109], v50 offset:26624\n\t"
        "ds_read_b64_tr_b16 a[110:111], v51 offset:26624\n\t"
        "s_waitcnt lgkmcnt(8)\n\t"
        "s_add_u32 m0, s35, 20480\n\t"
        "s_nop 0\n\t"
        "buffer_load_dwordx4 v10, s[64:67], s40 offen offset:0 lds\n\t"
        "buffer_load_dwordx4 v12, s[64:67], s40 offen offset:1024 lds\n\t"
        "buffer_load_dwordx4 v13, s[64:67], s40 offen offset:2048 lds\n\t"
        "buffer_load_dwordx4 v14, s[64:67], s40 offen offset:3072 lds\n\t"
        "s_waitcnt vmcnt(24)\n\t"
        "ds_read_b64_tr_b16 a[112:113], v48 offset:28672\n\t"
        "ds_read_b64_tr_b16 a[114:115], v49 offset:28672\n\t"
        "ds_read_b64_tr_b16 a[116:117], v48 offset:30720\n\t"
        "ds_read_b64_tr_b16 a[118:119], v49 offset:30720\n\t"
        "ds_read_b64_tr_b16 a[120:121], v50 offset:28672\n\t"
        "ds_read_b64_tr_b16 a[122:123], v51 offset:28672\n\t"
        "ds_read_b64_tr_b16 a[124:125], v50 offset:30720\n\t"
        "ds_read_b64_tr_b16 a[126:127], v51 offset:30720\n\t"
        "s_waitcnt lgkmcnt(8)\n\t"
        "s_add_u32 m0, s35, 24576\n\t"
        "s_nop 0\n\t"
        "buffer_load_dwordx4 v10, s[64:67], s41 offen offset:0 lds\n\t"
        "buffer_load_dwordx4 v12, s[64:67], s41 offen offset:1024 lds\n\t"
        "buffer_load_dwordx4 v13, s[64:67], s41 offen offset:2048 lds\n\t"
        "buffer_load_dwordx4 v14, s[64:67], s41 offen offset:3072 lds\n\t"
        "s_waitcnt vmcnt(24)\n\t"
        "ds_read_b64_tr_b16 a[128:129], v48 offset:0\n\t"
        "ds_read_b64_tr_b16 a[130:131], v49 offset:0\n\t"
        "ds_read_b64_tr_b16 a[132:133], v48 offset:2048\n\t"
        "ds_read_b64_tr_b16 a[134:135], v49 offset:2048\n\t"
        "ds_read_b64_tr_b16 a[136:137], v50 offset:0\n\t"
        "ds_read_b64_tr_b16 a[138:139], v51 offset:0\n\t"
        "ds_read_b64_tr_b16 a[140:141], v50 offset:2048\n\t"
        "ds_read_b64_tr_b16 a[142:143], v51 offset:2048\n\t"
        "s_waitcnt lgkmcnt(8)\n\t"
        "s_add_u32 m0, s35, 28672\n\t"
        "s_nop 0\n\t"
        "buffer_load_dwordx4 v10, s[64:67], s42 offen offset:0 lds\n\t"
        "buffer_load_dwordx4 v12, s[64:67], s42 offen offset:1024 lds\n\t"
        "buffer_load_dwordx4 v13, s[64:67], s42 offen offset:2048 lds\n\t"
        "buffer_load_dwordx4 v14, s[64:67], s42 offen offset:3072 lds\n\t"
        "s_waitcnt vmcnt(24)\n\t"
        "ds_read_b64_tr_b16 a[144:145], v48 offset:4096\n\t"
        "ds_read_b64_tr_b16 a[146:147], v49 offset:4096\n\t"
        "ds_read_b64_tr_b16 a[148:149], v48 offset:6144\n\t"
        "ds_read_b64_tr_b16 a[150:151], v49 offset:6144\n\t"
        "ds_read_b64_tr_b16 a[152:153], v50 offset:4096\n\t"
        "ds_read_b64_tr_b16 a[154:155], v51 offset:4096\n\t"
        "ds_read_b64_tr_b16 a[156:157], v50 offset:6144\n\t"
        "ds_read_b64_tr_b16 a[158:159], v51 offset:6144\n\t"
        "s_waitcnt lgkmcnt(8)\n\t"
        "s_add_u32 m0, s35, 0\n\t"
        "s_nop 0\n\t"
        "buffer_load_dwordx4 v10, s[64:67], s37 offen offset:0 lds\n\t"
        "buffer_load_dwordx4 v12, s[64:67], s37 offen offset:1024 lds\n\t"
        "buffer_load_dwordx4 v13, s[64:67], s37 offen offset:2048 lds\n\t"
        "buffer_load_dwordx4 v14, s[64:67], s37 offen offset:3072 lds\n\t"
        "s_waitcnt vmcnt(24)\n\t"
        "ds_read_b64_tr_b16 a[160:161], v48 offset:8192\n\t"
        "ds_read_b64_tr_b16 a[162:163], v49 offset:8192\n\t"
        "ds_read_b64_tr_b16 a[164:165], v48 offset:10240\n\t"
        "ds_read_b64_tr_b16 a[166:167], v49 offset:10240\n\t"
        "ds_read_b64_tr_b16 a[168:169], v50 offset:8192\n\t"
        "ds_read_b64_tr_b16 a[170:171], v51 offset:8192\n\t"
        "ds_read_b64_tr_b16 a[172:173], v50 offset:10240\n\t"
        "ds_read_b64_tr_b16 a[174:175], v51 offset:10240\n\t"
        "s_waitcnt lgkmcnt(8)\n\t"
        "s_add_u32 m0, s35, 4096\n\t"
        "s_nop 0\n\t"
        "buffer_load_dwordx4 v10, s[64:67], s38 offen offset:0 lds\n\t"
        "buffer_load_dwordx4 v12, s[64:67], s38 offen offset:1024 lds\n\t"
        "buffer_load_dwordx4 v13, s[64:67], s38 offen offset:2048 lds\n\t"
        "buffer_load_dwordx4 v14, s[64:67], s38 offen offset:3072 lds\n\t"
        "s_waitcnt vmcnt(24)\n\t"
        "ds_read_b64_tr_b16 a[176:177], v48 offset:12288\n\t"
        "ds_read_b64_tr_b16 a[178:179], v49 offset:12288\n\t"
        "ds_read_b64_tr_b16 a[180:181], v48 offset:14336\n\t"
        "ds_read_b64_tr_b16 a[182:183], v49 offset:14336\n\t"
        "ds_read_b64_tr_b16 a[184:185], v50 offset:12288\n\t"
        "ds_read_b64_tr_b16 a[186:187], v51 offset:12288\n\t"
        "ds_read_b64_tr_b16 a[188:189], v50 offset:14336\n\t"
        "ds_read_b64_tr_b16 a[190:191], v51 offset:14336\n\t"
        "s_waitcnt lgkmcnt(8)\n\t"
        "s_add_u32 m0, s35, 8192\n\t"
        "s_nop 0\n\t"
        "buffer_load_dwordx4 v10, s[68:71], s39 offen offset:0 lds\n\t"
        "buffer_load_dwordx4 v12, s[68:71], s39 offen offset:1024 lds\n\t"
        "buffer_load_dwordx4 v13, s[68:71], s39 offen offset:2048 lds\n\t"
        "buffer_load_dwordx4 v14, s[68:71], s39 offen offset:3072 lds\n\t"
        "s_waitcnt vmcnt(24)\n\t"
        "ds_read_b64_tr_b16 a[192:193], v48 offset:16384\n\t"
        "ds_read_b64_tr_b16 a[194:195], v49 offset:16384\n\t"
        "ds_read_b64_tr_b16 a[196:197], v48 offset:18432\n\t"
        "ds_read_b64_tr_b16 a[198:199], v49 offset:18432\n\t"
        "ds_read_b64_tr_b16 a[200:201], v50 offset:16384\n\t"
        "ds_read_b64_tr_b16 a[202:203], v51 offset:16384\n\t"
        "ds_read_b64_tr_b16 a[204:205], v50 offset:18432\n\t"
        "ds_read_b64_tr_b16 a[206:207], v51 offset:18432\n\t"
        "s_waitcnt lgkmcnt(8)\n\t"
        "s_add_u32 m0, s35, 12288\n\t"
        "s_nop 0\n\t"
        "buffer_load_dwordx4 v10, s[68:71], s40 offen offset:0 lds\n\t"
        "buffer_load_dwordx4 v12, s[68:71], s40 offen offset:1024 lds\n\t"
        "buffer_load_dwordx4 v13, s[68:71], s40 offen offset:2048 lds\n\t"
        "buffer_load_dwordx4 v14, s[68:71], s40 offen offset:3072 lds\n\t"
        "s_waitcnt vmcnt(24)\n\t"
        "ds_read_b64_tr_b16 a[208:209], v48 offset:20480\n\t"
        "ds_read_b64_tr_b16 a[210:211], v49 offset:20480\n\t"
        "ds_read_b64_tr_b16 a[212:213], v48 offset:22528\n\t"
        "ds_read_b64_tr_b16 a[214:215], v49 offset:22528\n\t"
        "ds_read_b64_tr_b16 a[216:217], v50 offset:20480\n\t"
        "ds_read_b64_tr_b16 a[218:219], v51 offset:20480\n\t"
        "ds_read_b64_tr_b16 a[220:221], v50 offset:22528\n\t"
        "ds_read_b64_tr_b16 a[222:223], v51 offset:22528\n\t"
        "s_waitcnt lgkmcnt(8)\n\t"
        "s_add_u32 m0, s35, 16384\n\t"
        "s_nop 0\n\t"
        "buffer_load_dwordx4 v10, s[64:67], s43 offen offset:0 lds\n\t"
        "buffer_load_dwordx4 v12, s[64:67], s43 offen offset:1024 lds\n\t"
        "buffer_load_dwordx4 v13, s[64:67], s43 offen offset:2048 lds\n\t"
        "buffer_load_dwordx4 v14, s[64:67], s43 offen offset:3072 lds\n\t"
        "s_waitcnt vmcnt(24)\n\t"
        "ds_read_b64_tr_b16 a[224:225], v48 offset:24576\n\t"
        "ds_read_b64_tr_b16 a[226:227], v49 offset:24576\n\t"
        "ds_read_b64_tr_b16 a[228:229], v48 offset:26624\n\t"
        "ds_read_b64_tr_b16 a[230:231], v49 offset:26624\n\t"
        "ds_read_b64_tr_b16 a[232:233], v50 offset:24576\n\t"
        "ds_read_b64_tr_b16 a[234:235], v51 offset:24576\n\t"
        "ds_read_b64_tr_b16 a[236:237], v50 offset:26624\n\t"
        "ds_read_b64_tr_b16 a[238:239], v51 offset:26624\n\t"
        "s_waitcnt lgkmcnt(8)\n\t"
        "s_add_u32 m0, s35, 20480\n\t"
        "s_nop 0\n\t"
        "buffer_load_dwordx4 v10, s[72:75], s36 offen offset:0 lds\n\t"
        "buffer_load_dwordx4 v12, s[72:75], s36 offen offset:1024 lds\n\t"
        "buffer_load_dwordx4 v13, s[72:75], s36 offen offset:2048 lds\n\t"
        "buffer_load_dwordx4 v14, s[72:75], s36 offen offset:3072 lds\n\t"
        "s_waitcnt vmcnt(24)\n\t"
        "ds_read_b64_tr_b16 a[240:241], v48 offset:28672\n\t"
        "ds_read_b64_tr_b16 a[242:243], v49 offset:28672\n\t"
        "ds_read_b64_tr_b16 a[244:245], v48 offset:30720\n\t"
        "ds_read_b64_tr_b16 a[246:247], v49 offset:30720\n\t"
        "ds_read_b64_tr_b16 a[248:249], v50 offset:28672\n\t"
        "ds_read_b64_tr_b16 a[250:251], v51 offset:28672\n\t"
        "ds_read_b64_tr_b16 a[252:253], v50 offset:30720\n\t"
        "ds_read_b64_tr_b16 a[254:255], v51 offset:30720\n\t"
        "s_waitcnt lgkmcnt(8)\n\t"
        "s_add_u32 m0, s35, 24576\n\t"
        "s_nop 0\n\t"
        "buffer_load_dwordx4 v10, s[72:75], s39 offen offset:0 lds\n\t"
        "buffer_load_dwordx4 v12, s[72:75], s39 offen offset:1024 lds\n\t"
        "buffer_load_dwordx4 v13, s[72:75], s39 offen offset:2048 lds\n\t"
        "buffer_load_dwordx4 v14, s[72:75], s39 offen offset:3072 lds\n\t"
        "s_waitcnt vmcnt(24)\n\t"
        "ds_read_b64_tr_b16 v[52:53], v48 offset:0\n\t"
        "ds_read_b64_tr_b16 v[54:55], v49 offset:0\n\t"
        "ds_read_b64_tr_b16 v[56:57], v48 offset:2048\n\t"
        "ds_read_b64_tr_b16 v[58:59], v49 offset:2048\n\t"
        "ds_read_b64_tr_b16 v[128:129], v50 offset:0\n\t"
        "ds_read_b64_tr_b16 v[130:131], v51 offset:0\n\t"
        "ds_read_b64_tr_b16 v[132:133], v50 offset:2048\n\t"
        "ds_read_b64_tr_b16 v[134:135], v51 offset:2048\n\t"
        "s_cmp_eq_u32 s34, 0\n\t"
        "s_cbranch_scc1 Lh_b2_%=\n\t"
        "s_mov_b32 s49, 0\n\t"
        "Lh_b1_%=:\n\t"
        "buffer_load_dword v24, v15, s[8:11], s34 offen sc1\n\t"
        "s_waitcnt vmcnt(0)\n\t"
        "v_readfirstlane_b32 s53, v24\n\t"
        "s_add_u32 s49, s49, 1\n\t"
        "s_cmp_gt_u32 s49, 0x40000\n\t"
        "s_cbranch_scc1 Lh_end_%=\n\t"
        "s_cmp_lg_u32 s53, s63\n\t"
        "s_cbranch_scc1 Lh_b1_%=\n\t"
        "v_mov_b32 v24, 1\n\t"
        "s_sub_u32 s53, s34, 884736\n\t"
        "s_lshr_b32 s53, s53, 5\n\t"
        "s_add_u32 s53, s53, 917504\n\t"
        "buffer_store_dword v24, v15, s[8:11], s53 offen sc1\n\t"
        "Lh_b2_%=:\n\t"
        "s_waitcnt lgkmcnt(8)\n\t"
        "s_add_u32 m0, s35, 28672\n\t"
        "s_nop 0\n\t"
        "buffer_load_dwordx4 v10, s[72:75], s42 offen offset:0 lds\n\t"
        "buffer_load_dwordx4 v12, s[72:75], s42 offen offset:1024 lds\n\t"
        "buffer_load_dwordx4 v13, s[72:75], s42 offen offset:2048 lds\n\t"
        "buffer_load_dwordx4 v14, s[72:75], s42 offen offset:3072 lds\n\t"
        "s_waitcnt vmcnt(24)\n\t"
        "ds_read_b64_tr_b16 v[16:17], v48 offset:4096\n\t"
        "ds_read_b64_tr_b16 v[18:19], v49 offset:4096\n\t"
        "ds_read_b64_tr_b16 v[20:21], v48 offset:6144\n\t"
        "ds_read_b64_tr_b16 v[22:23], v49 offset:6144\n\t"
        "ds_read_b64_tr_b16 v[136:137], v50 offset:4096\n\t"
        "ds_read_b64_tr_b16 v[138:139], v51 offset:4096\n\t"
        "ds_read_b64_tr_b16 v[140:141], v50 offset:6144\n\t"
        "ds_read_b64_tr_b16 v[142:143], v51 offset:6144\n\t"
        "s_waitcnt lgkmcnt(8)\n\t"
        "ds_write_b128 v6, v[52:55] offset:0\n\t"
        "ds_write_b128 v6, v[56:59] offset:1024\n\t"
        "s_waitcnt vmcnt(20)\n\t"
        "ds_read_b64_tr_b16 v[52:53], v48 offset:8192\n\t"
        "ds_read_b64_tr_b16 v[54:55], v49 offset:8192\n\t"
        "ds_read_b64_tr_b16 v[56:57], v48 offset:10240\n\t"
        "ds_read_b64_tr_b16 v[58:59], v49 offset:10240\n\t"
        "ds_read_b64_tr_b16 v[144:145], v50 offset:8192\n\t"
        "ds_read_b64_tr_b16 v[146:147], v51 offset:8192\n\t"
        "ds_read_b64_tr_b16 v[148:149], v50 offset:10240\n\t"
        "ds_read_b64_tr_b16 v[150:151], v51 offset:10240\n\t"
        "s_waitcnt lgkmcnt(10)\n\t"
        "ds_write_b128 v6, v[16:19] offset:2048\n\t"
        "ds_write_b128 v6, v[20:23] offset:3072\n\t"
        "s_waitcnt vmcnt(16)\n\t"
        "ds_read_b64_tr_b16 v[16:17], v48 offset:12288\n\t"
        "ds_read_b64_tr_b16 v[18:19], v49 offset:12288\n\t"
        "ds_read_b64_tr_b16 v[20:21], v48 offset:14336\n\t"
        "ds_read_b64_tr_b16 v[22:23], v49 offset:14336\n\t"
        "ds_read_b64_tr_b16 v[152:153], v50 offset:12288\n\t"
        "ds_read_b64_tr_b16 v[154:155], v51 offset:12288\n\t"
        "ds_read_b64_tr_b16 v[156:157], v50 offset:14336\n\t"
        "ds_read_b64_tr_b16 v[158:159], v51 offset:14336\n\t"
        "s_waitcnt lgkmcnt(10)\n\t"
        "ds_write_b128 v6, v[52:55] offset:4096\n\t"
        "ds_write_b128 v6, v[56:59] offset:5120\n\t"
        "s_waitcnt vmcnt(12)\n\t"
        "ds_read_b64_tr_b16 v[52:53], v48 offset:16384\n\t"
        "ds_read_b64_tr_b16 v[54:55], v49 offset:16384\n\t"
        "ds_read_b64_tr_b16 v[56:57], v48 offset:18432\n\t"
        "ds_read_b64_tr_b16 v[58:59], v49 offset:18432\n\t"
        "ds_read_b64_tr_b16 v[160:161], v50 offset:16384\n\t"
        "ds_read_b64_tr_b16 v[162:163], v51 offset:16384\n\t"
        "ds_read_b64_tr_b16 v[164:165], v50 offset:18432\n\t"
        "ds_read_b64_tr_b16 v[166:167], v51 offset:18432\n\t"
        "s_waitcnt lgkmcnt(10)\n\t"
        "ds_write_b128 v6, v[16:19] offset:6144\n\t"
        "ds_write_b128 v6, v[20:23] offset:7168\n\t"
        "s_waitcnt vmcnt(8)\n\t"
        "ds_read_b64_tr_b16 v[16:17], v48 offset:20480\n\t"
        "ds_read_b64_tr_b16 v[18:19], v49 offset:20480\n\t"
        "ds_read_b64_tr_b16 v[20:21], v48 offset:22528\n\t"
        "ds_read_b64_tr_b16 v[22:23], v49 offset:22528\n\t"
        "ds_read_b64_tr_b16 v[168:169], v50 offset:20480\n\t"
        "ds_read_b64_tr_b16 v[170:171], v51 offset:20480\n\t"
        "ds_read_b64_tr_b16 v[172:173], v50 offset:22528\n\t"
        "ds_read_b64_tr_b16 v[174:175], v51 offset:22528\n\t"
        "s_waitcnt lgkmcnt(10)\n\t"
        "ds_write_b128 v6, v[52:55] offset:8192\n\t"
        "ds_write_b128 v6, v[56:59] offset:9216\n\t"
        "s_waitcnt vmcnt(4)\n\t"
        "ds_read_b64_tr_b16 v[52:53], v48 offset:24576\n\t"
        "ds_read_b64_tr_b16 v[54:55], v49 offset:24576\n\t"
        "ds_read_b64_tr_b16 v[56:57], v48 offset:26624\n\t"
        "ds_read_b64_tr_b16 v[58:59], v49 offset:26624\n\t"
        "ds_read_b64_tr_b16 v[176:177], v50 offset:24576\n\t"
        "ds_read_b64_tr_b16 v[178:179], v51 offset:24576\n\t"
        "ds_read_b64_tr_b16 v[180:181], v50 offset:26624\n\t"
        "ds_read_b64_tr_b16 v[182:183], v51 offset:26624\n\t"
        "s_waitcnt lgkmcnt(10)\n\t"
        "ds_write_b128 v6, v[16:19] offset:10240\n\t"
        "ds_write_b128 v6, v[20:23] offset:11264\n\t"
        "s_waitcnt vmcnt(0)\n\t"
        "ds_read_b64_tr_b16 v[16:17], v48 offset:28672\n\t"
        "ds_read_b64_tr_b16 v[18:19], v49 offset:28672\n\t"
        "ds_read_b64_tr_b16 v[20:21], v48 offset:30720\n\t"
        "ds_read_b64_tr_b16 v[22:23], v49 offset:30720\n\t"
        "ds_read_b64_tr_b16 v[184:185], v50 offset:28672\n\t"
        "ds_read_b64_tr_b16 v[186:187], v51 offset:28672\n\t"
        "ds_read_b64_tr_b16 v[188:189], v50 offset:30720\n\t"
        "ds_read_b64_tr_b16 v[190:191], v51 offset:30720\n\t"
        "s_waitcnt lgkmcnt(10)\n\t"
        "ds_write_b128 v6, v[52:55] offset:12288\n\t"
        "ds_write_b128 v6, v[56:59] offset:13312\n\t"
        "s_barrier\n\t"
        "s_waitcnt lgkmcnt(2)\n\t"
        "ds_write_b128 v6, v[16:19] offset:14336\n\t"
        "ds_write_b128 v6, v[20:23] offset:15360\n\t"
        "ds_write_b128 v6, v[64:67] offset:16384\n\t"
        "ds_write_b128 v6, v[68:71] offset:17408\n\t"
        "ds_write_b128 v6, v[72:75] offset:18432\n\t"
        "ds_write_b128 v6, v[76:79] offset:19456\n\t"
        "ds_write_b128 v6, v[80:83] offset:20480\n\t"
        "ds_write_b128 v6, v[84:87] offset:21504\n\t"
        "ds_write_b128 v6, v[88:91] offset:22528\n\t"
        "ds_write_b128 v6, v[92:95] offset:23552\n\t"
        "ds_write_b128 v6, v[96:99] offset:24576\n\t"
        "ds_write_b128 v6, v[100:103] offset:25600\n\t"
        "ds_write_b128 v6, v[104:107] offset:26624\n\t"
        "ds_write_b128 v6, v[108:111] offset:27648\n\t"
        "ds_write_b128 v6, v[112:115] offset:28672\n\t"
        "ds_write_b128 v6, v[116:119] offset:29696\n\t"
        "ds_write_b128 v6, v[120:123] offset:30720\n\t"
        "ds_write_b128 v6, v[124:127] offset:31744\n\t"
        "v_mov_b32 v13, 0\n\t"
        "v_lshlrev_b32 v16, 3, v0\n\t"
        "s_mov_b32 s49, 0\n\t"
        "s_mov_b32 s54, 926720\n\t"
        "Lh_b3_%=:\n\t"
        "buffer_load_dword v17, v16, s[8:11], s54 offen sc1\n\t"
        "buffer_load_dword v18, v13, s[8:11], s29 offen sc1\n\t"
        "s_waitcnt vmcnt(0)\n\t"
        "v_cmp_eq_u32 vcc, 0, v17\n\t"
        "s_add_u32 s49, s49, 1\n\t"
        "s_cmp_gt_u32 s49, 0x40000\n\t"
        "s_cbranch_scc1 Lh_end_%=\n\t"
        "s_cbranch_vccnz Lh_b3_%=\n\t"
        "ds_write_b32 v13, v17 offset:32512\n\t"
        "v_readfirstlane_b32 s34, v17\n\t"
        "v_readfirstlane_b32 s55, v18\n\t"
        "s_and_b32 s53, s34, 1\n\t"
        "s_lshl_b32 s53, s53, 13\n\t"
        "s_xor_b32 s20, s20, s53\n\t"
        "s_xor_b32 s21, s21, s53\n\t"
        "s_xor_b32 s22, s22, s53\n\t"
        "s_xor_b32 s26, s26, s53\n\t"
        "s_xor_b32 s30, s30, s53\n\t"
        "s_xor_b32 s31, s31, s53\n\t"
        "s_cmp_eq_u32 s5, 0\n\t"
        "s_cselect_b32 s53, 0, s53\n\t"
        "s_xor_b32 s5, s5, s53\n\t"
        "s_mov_b32 s40, s20\n\t"
        "s_mov_b32 s41, 0\n\t"
        "s_mov_b32 s42, s21\n\t"
        "s_add_u32 s43, s42, 65536\n\t"
        "s_mov_b32 s44, s22\n\t"
        "s_mov_b32 s73, s44\n\t"
        "s_add_u32 s45, s44, 16384\n\t"
        "s_mov_b32 s47, s26\n\t"
        "s_mov_b32 s52, 0x3e0293ee\n\t"
        "v_mov_b32 v12, 0\n\t"
        "v_mov_b32 v13, 0\n\t"
        "v_mov_b32 v14, 0\n\t"
        "s_mov_b32 s56, 1\n\t"
        "s_mov_b32 s57, s6\n\t"
        "s_mov_b32 s50, 0xffff\n\t"
        "s_mov_b32 s51, 0xffff\n\t"
        "s_waitcnt lgkmcnt(0)\n\t"
        "v_mov_b32 v20, -1\n\t"
        "v_mov_b32 v21, -1\n\t"
        "v_mov_b32 v22, -1\n\t"
        "v_mov_b32 v23, -1\n\t"
        "s_add_u32 s53, s47, 983040\n\t"
        "s_add_u32 s54, s53, s27\n\t"
        "buffer_store_dwordx4 v[20:23], v0, s[16:19], s53 offen sc1\n\t"
        "buffer_store_dwordx2 v[20:21], v4, s[16:19], s54 offen sc1\n\t"
        "s_add_u32 s53, s47, 999424\n\t"
        "s_add_u32 s54, s53, s27\n\t"
        "buffer_store_dwordx4 v[20:23], v0, s[16:19], s53 offen sc1\n\t"
        "buffer_store_dwordx2 v[20:21], v4, s[16:19], s54 offen sc1\n\t"
        "s_add_u32 s53, s47, 1015808\n\t"
        "s_add_u32 s54, s53, s27\n\t"
        "buffer_store_dwordx4 v[20:23], v0, s[16:19], s53 offen sc1\n\t"
        "buffer_store_dwordx2 v[20:21], v4, s[16:19], s54 offen sc1\n\t"
        "s_add_u32 s53, s47, 1032192\n\t"
        "s_add_u32 s54, s53, s27\n\t"
        "buffer_store_dwordx4 v[20:23], v0, s[16:19], s53 offen sc1\n\t"
        "buffer_store_dwordx2 v[20:21], v4, s[16:19], s54 offen sc1\n\t"
        "s_cmp_ge_u32 s55, s56\n\t"
        "s_cbranch_scc1 Lh_kv1b_%=\n\t"
        "s_mov_b32 s49, 0\n\t"
        "Lh_kv1a_%=:\n\t"
        "buffer_load_dword v11, v13, s[8:11], s29 offen sc1\n\t"
        "s_waitcnt vmcnt(0)\n\t"
        "v_readfirstlane_b32 s55, v11\n\t"
        "s_add_u32 s49, s49, 1\n\t"
        "s_cmp_gt_u32 s49, 0x40000\n\t"
        "s_cbranch_scc1 Lh_end_%=\n\t"
        "s_sleep 1\n\t"
        "s_cmp_lt_u32 s55, s56\n\t"
        "s_cbranch_scc1 Lh_kv1a_%=\n\t"
        "Lh_kv1b_%=:\n\t"
        "s_mov_b32 m0, s28\n\t"
        "s_nop 0\n\t"
        "buffer_load_dwordx4 v0, s[12:15], s73 offen sc1 lds\n\t"
        "buffer_load_dwordx4 v0, s[12:15], s73 offen offset:1024 sc1 lds\n\t"
        "s_lshl_b32 m0, s25, 10\n\t"
        "s_nop 0\n\t"
        "buffer_load_dwordx4 v0, s[12:15], s45 offen sc1 lds\n\t"
        "buffer_load_dwordx4 v0, s[12:15], s45 offen offset:1024 sc1 lds\n\t"
        "s_memrealtime s[78:79]\n\t"
        "s_memtime s[74:75]\n\t"
        "s_waitcnt vmcnt(0)\n\t"
        "s_waitcnt lgkmcnt(0)\n\t"
        "s_mov_b32 s79, s74\n\t"
        "v_mov_b32 v11, 0\n\t"
        "v_mov_b32 v15, 0\n\t"
        "Lh_step_%=:\n\t"
        "s_add_u32 s56, s56, 1\n\t"
        "s_min_u32 s56, s56, 64\n\t"
        "s_add_u32 s44, s44, 0x8000\n\t"
        "s_add_u32 s53, s25, 0\n\t"
        "s_and_b32 s53, s53, 7\n\t"
        "s_lshl_b32 s53, s53, 10\n\t"
        "s_add_u32 s36, s40, s53\n\t"
        "s_add_u32 s53, s25, 2\n\t"
        "s_and_b32 s53, s53, 7\n\t"
        "s_lshl_b32 s53, s53, 10\n\t"
        "s_add_u32 s37, s40, s53\n\t"
        "s_add_u32 s53, s25, 4\n\t"
        "s_and_b32 s53, s53, 7\n\t"
        "s_lshl_b32 s53, s53, 10\n\t"
        "s_add_u32 s38, s40, s53\n\t"
        "s_add_u32 s53, s25, 6\n\t"
        "s_and_b32 s53, s53, 7\n\t"
        "s_lshl_b32 s53, s53, 10\n\t"
        "s_add_u32 s39, s40, s53\n\t"
        "s_mov_b32 s49, 0\n\t"
        "Lh_redo_%=:\n\t"
        "ds_read_b128 v[112:115], v6 offset:0\n\t"
        "ds_read_b128 v[116:119], v6 offset:1024\n\t"
        "ds_read_b128 v[120:123], v6 offset:2048\n\t"
        "ds_read_b128 v[124:127], v6 offset:3072\n\t"
        "v_mov_b64 v[48:49], 0\n\t"
        "v_mov_b64 v[50:51], 0\n\t"
        "v_mov_b64 v[52:53], 0\n\t"
        "v_mov_b64 v[54:55], 0\n\t"
        "v_mov_b64 v[56:57], 0\n\t"
        "v_mov_b64 v[58:59], 0\n\t"
        "v_mov_b64 v[60:61], 0\n\t"
        "v_mov_b64 v[62:63], 0\n\t"
        "v_mov_b64 v[64:65], 0\n\t"
        "v_mov_b64 v[66:67], 0\n\t"
        "v_mov_b64 v[68:69], 0\n\t"
        "v_mov_b64 v[70:71], 0\n\t"
        "s_cmp_lg_u32 s49, 0\n\t"
        "s_cbranch_scc1 Lh_clk_%=\n\t"
        "Lh_dec_%=:\n\t"
        "v_readlane_b32 s54, v11, 0\n\t"
        "s_max_u32 s57, s57, s54\n\t"
        "v_readlane_b32 s54, v11, 1\n\t"
        "s_max_u32 s57, s57, s54\n\t"
        "v_readlane_b32 s54, v11, 2\n\t"
        "s_max_u32 s57, s57, s54\n\t"
        "v_readlane_b32 s54, v11, 3\n\t"
        "s_max_u32 s57, s57, s54\n\t"
        "s_bfe_u32 s58, s57, 0x80017\n\t"
        "s_bfe_u32 s46, s57, 0x60011\n\t"
        "s_add_u32 s46, s46, s58\n\t"
        "s_mul_i32 s58, s46, s41\n\t"
        "s_add_u32 s58, s58, s34\n\t"
        "s_and_b32 s59, s57, 0x1ffff\n\t"
        "s_add_u32 s59, s59, s58\n\t"
        "Lh_clk_%=:\n\t"
        "s_memrealtime s[60:61]\n\t"
        "s_waitcnt lgkmcnt(0)\n\t"
        "s_sub_u32 s53, s59, s60\n\t"
        "s_cmp_lt_i32 s53, -4\n\t"
        "s_cbranch_scc1 Lh_late_%=\n\t"
        "s_cmp_gt_i32 s53, 2000\n\t"
        "s_cbranch_scc1 Lh_poll_%=\n\t"
        "s_cmp_gt_i32 s53, 14\n\t"
        "s_cbranch_scc1 Lh_dec_%=\n\t"
        "s_cmp_lt_i32 s53, 4\n\t"
        "s_cbranch_scc1 Lh_fine_%=\n\t"
        "Lh_tw_%=:\n\t"
        "s_memrealtime s[60:61]\n\t"
        "s_waitcnt lgkmcnt(0)\n\t"
        "s_sub_u32 s53, s59, s60\n\t"
        "s_cmp_gt_i32 s53, 3\n\t"
        "s_cbranch_scc1 Lh_tw_%=\n\t"
        "Lh_fine_%=:\n\t"
        "s_cmp_lt_i32 s53, 1\n\t"
        "s_cbranch_scc1 Lh_poll_%=\n\t"
        "s_nop 3\n\t"
        "s_cmp_lt_i32 s53, 2\n\t"
        "s_cbranch_scc1 Lh_poll_%=\n\t"
        "s_nop 3\n\t"
        "s_cmp_lt_i32 s53, 3\n\t"
        "s_cbranch_scc1 Lh_poll_%=\n\t"
        "s_nop 3\n\t"
        "Lh_poll_%=:\n\t"
        "s_memtime s[74:75]\n\t"
        "buffer_load_dwordx4 v[16:19], v1, s[8:11], s36 offen offset:0 sc1\n\t"
        "buffer_load_dwordx4 v[20:23], v1, s[8:11], s37 offen offset:0 sc1\n\t"
        "s_cmp_ge_u32 s49, 2\n\t"
        "s_cbranch_scc1 Lh_g10_%=\n\t"
        "s_waitcnt vmcnt(6)\n\t"
        "s_barrier\n\t"
        "v_readfirstlane_b32 s53, v15\n\t"
        "s_max_u32 s55, s55, s53\n\t"
        "s_cmp_ge_u32 s55, s56\n\t"
        "s_cselect_b32 s73, s44, 0x40000000\n\t"
        "s_add_u32 s45, s73, 16384\n\t"
        "s_add_u32 s53, s56, 3\n\t"
        "s_min_u32 s53, s53, 64\n\t"
        "s_cmp_lt_u32 s55, s53\n\t"
        "s_cselect_b32 s72, s29, 0x40000000\n\t"
        "s_nop 6\n\t"
        "Lh_g10_%=:\n\t"
        "buffer_load_dwordx4 v[24:27], v1, s[8:11], s38 offen offset:0 sc1\n\t"
        "buffer_load_dwordx4 v[28:31], v1, s[8:11], s39 offen offset:0 sc1\n\t"
        "s_cmp_ge_u32 s49, 2\n\t"
        "s_cbranch_scc1 Lh_g11_%=\n\t"
        "s_and_b32 s53, s28, 0x6000\n\t"
        "v_add_u32 v10, s53, v0\n\t"
        "ds_read_b128 v[72:75], v10 offset:0\n\t"
        "ds_read_b128 v[76:79], v10 offset:1024\n\t"
        "ds_read_b128 v[80:83], v10 offset:2048\n\t"
        "ds_read_b128 v[84:87], v10 offset:3072\n\t"
        "ds_read_b128 v[88:91], v10 offset:4096\n\t"
        "ds_read_b128 v[92:95], v10 offset:5120\n\t"
        "ds_read_b128 v[96:99], v10 offset:6144\n\t"
        "ds_read_b128 v[100:103], v10 offset:7168\n\t"
        "ds_read_b128 v[104:107], v2 offset:0\n\t"
        "ds_read_b128 v[108:111], v2 offset:1024\n\t"
        "s_xor_b32 s28, s28, 0x6000\n\t"
        "s_nop 4\n\t"
        "Lh_g11_%=:\n\t"
        "buffer_load_dwordx4 v[32:35], v1, s[8:11], s36 offen offset:1024 sc1\n\t"
        "buffer_load_dwordx4 v[36:39], v1, s[8:11], s37 offen offset:1024 sc1\n\t"
        "s_cmp_ge_u32 s49, 2\n\t"
        "s_cbranch_scc1 Lh_g12_%=\n\t"
        "s_nop 9\n\t"
        "Lh_g12_%=:\n\t"
        "buffer_load_dwordx4 v[40:43], v1, s[8:11], s38 offen offset:1024 sc1\n\t"
        "buffer_load_dwordx4 v[44:47], v1, s[8:11], s39 offen offset:1024 sc1\n\t"
        "s_mov_b32 m0, s28\n\t"
        "Lh_s1_%=:\n\t"
        "s_waitcnt vmcnt(7)\n\t"
        "v_smfmac_f32_16x16x64_bf16 v[48:51], v[16:19], a[176:183], v7\n\t"
        "s_memtime s[76:77]\n\t"
        "v_smfmac_f32_16x16x64_bf16 v[52:55], v[16:19], a[184:191], v7\n\t"
        "s_waitcnt vmcnt(6)\n\t"
        "v_smfmac_f32_16x16x64_bf16 v[48:51], v[20:23], v[112:119], v7\n\t"
        "ds_read_b128 v[112:115], v6 offset:4096\n\t"
        "v_smfmac_f32_16x16x64_bf16 v[52:55], v[20:23], v[128:135], v7\n\t"
        "ds_read_b128 v[116:119], v6 offset:5120\n\t"
        "v_smfmac_f32_16x16x64_bf16 v[56:59], v[16:19], a[0:7], v7\n\t"
        "v_smfmac_f32_16x16x64_bf16 v[60:63], v[16:19], a[8:15], v7\n\t"
        "v_smfmac_f32_16x16x64_bf16 v[56:59], v[20:23], a[16:23], v7\n\t"
        "v_smfmac_f32_16x16x64_bf16 v[60:63], v[20:23], a[24:31], v7\n\t"
        "s_waitcnt vmcnt(5)\n\t"
        "v_smfmac_f32_16x16x64_bf16 v[48:51], v[24:27], v[120:127], v7\n\t"
        "ds_read_b128 v[120:123], v6 offset:6144\n\t"
        "v_smfmac_f32_16x16x64_bf16 v[52:55], v[24:27], v[136:143], v7\n\t"
        "s_waitcnt vmcnt(4)\n\t"
        "v_smfmac_f32_16x16x64_bf16 v[48:51], v[28:31], a[192:199], v7\n\t"
        "ds_read_b128 v[124:127], v6 offset:7168\n\t"
        "v_smfmac_f32_16x16x64_bf16 v[52:55], v[28:31], a[200:207], v7\n\t"
        "v_smfmac_f32_16x16x64_bf16 v[56:59], v[24:27], a[32:39], v7\n\t"
        "v_smfmac_f32_16x16x64_bf16 v[60:63], v[24:27], a[40:47], v7\n\t"
        "s_waitcnt lgkmcnt(2)\n\t"
        "v_smfmac_f32_16x16x64_bf16 v[56:59], v[28:31], v[112:119], v7\n\t"
        "ds_read_b128 v[112:115], v6 offset:8192\n\t"
        "v_smfmac_f32_16x16x64_bf16 v[60:63], v[28:31], v[144:151], v7\n\t"
        "s_waitcnt vmcnt(3)\n\t"
        "v_smfmac_f32_16x16x64_bf16 v[48:51], v[32:35], a[208:215], v7\n\t"
        "ds_read_b128 v[116:119], v6 offset:9216\n\t"
        "v_smfmac_f32_16x16x64_bf16 v[52:55], v[32:35], a[216:223], v7\n\t"
        "s_waitcnt vmcnt(2)\n\t"
        "v_smfmac_f32_16x16x64_bf16 v[48:51], v[36:39], a[224:231], v7\n\t"
        "buffer_load_dwordx4 v0, s[12:15], s73 offen sc1 lds\n\t"
        "v_smfmac_f32_16x16x64_bf16 v[52:55], v[36:39], a[232:239], v7\n\t"
        "s_waitcnt lgkmcnt(2)\n\t"
        "v_smfmac_f32_16x16x64_bf16 v[56:59], v[32:35], v[120:127], v7\n\t"
        "ds_read_b128 v[120:123], v6 offset:10240\n\t"
        "v_smfmac_f32_16x16x64_bf16 v[60:63], v[32:35], v[152:159], v7\n\t"
        "ds_read_b128 v[124:127], v6 offset:11264\n\t"
        "v_smfmac_f32_16x16x64_bf16 v[56:59], v[36:39], a[48:55], v7\n\t"
        "buffer_load_dwordx4 v0, s[12:15], s73 offen offset:1024 sc1 lds\n\t"
        "s_lshl_b32 m0, s25, 10\n\t"
        "v_smfmac_f32_16x16x64_bf16 v[60:63], v[36:39], a[56:63], v7\n\t"
        "s_waitcnt vmcnt(3)\n\t"
        "v_smfmac_f32_16x16x64_bf16 v[48:51], v[40:43], a[240:247], v7\n\t"
        "buffer_load_dwordx4 v0, s[12:15], s45 offen sc1 lds\n\t"
        "v_smfmac_f32_16x16x64_bf16 v[52:55], v[40:43], a[248:255], v7\n\t"
        "s_waitcnt vmcnt(3)\n\t"
        "s_waitcnt lgkmcnt(2)\n\t"
        "v_smfmac_f32_16x16x64_bf16 v[48:51], v[44:47], v[112:119], v7\n\t"
        "ds_read_b128 v[112:115], v6 offset:12288\n\t"
        "v_smfmac_f32_16x16x64_bf16 v[52:55], v[44:47], v[160:167], v7\n\t"
        "ds_read_b128 v[116:119], v6 offset:13312\n\t"
        "v_smfmac_f32_16x16x64_bf16 v[56:59], v[40:43], a[64:71], v7\n\t"
        "v_add_f32 v48, v48, v50\n\t"
        "v_smfmac_f32_16x16x64_bf16 v[60:63], v[40:43], a[72:79], v7\n\t"
        "v_add_f32 v49, v49, v51\n\t"
        "v_smfmac_f32_16x16x64_bf16 v[56:59], v[44:47], a[80:87], v7\n\t"
        "v_cmp_u_f32 vcc, v48, v49\n\t"
        "v_smfmac_f32_16x16x64_bf16 v[60:63], v[44:47], a[88:95], v7\n\t"
        "s_waitcnt lgkmcnt(2)\n\t"
        "v_add_f32 v52, v52, v54\n\t"
        "v_smfmac_f32_16x16x64_bf16 v[64:67], v[16:19], v[120:127], v7\n\t"
        "s_cbranch_vccnz Lh_miss_%=\n\t"
        "Lh_cont_%=:\n\t"
        "v_add_f32 v53, v53, v55\n\t"
        "v_cvt_pk_bf16_f32 v51, v48, v49\n\t"
        "v_cvt_pk_bf16_f32 v55, v52, v53\n\t"
        "v_add_f32 v56, v56, v58\n\t"
        "v_add_f32 v57, v57, v59\n\t"
        "v_smfmac_f32_16x16x64_bf16 v[68:71], v[16:19], v[168:175], v7\n\t"
        "v_add_f32 v60, v60, v62\n\t"
        "v_add_f32 v61, v61, v63\n\t"
        "v_cvt_pk_bf16_f32 v50, v56, v57\n\t"
        "v_cvt_pk_bf16_f32 v54, v60, v61\n\t"
        "ds_write2_b64 v3, v[50:51], v[54:55] offset0:0 offset1:32\n\t"
        "v_smfmac_f32_16x16x64_bf16 v[64:67], v[20:23], a[96:103], v7\n\t"
        "ds_read_b128 v[120:123], v6 offset:14336\n\t"
        "v_smfmac_f32_16x16x64_bf16 v[68:71], v[20:23], a[104:111], v7\n\t"
        "ds_read_b128 v[124:127], v6 offset:15360\n\t"
        "s_cmp_ge_u32 s41, s4\n\t"
        "v_smfmac_f32_16x16x64_bf16 v[64:67], v[24:27], a[112:119], v7\n\t"
        "buffer_load_dwordx4 v0, s[12:15], s45 offen offset:1024 sc1 lds\n\t"
        "s_cbranch_scc1 Lh_cal_%=\n\t"
        "Lh_calr_%=:\n\t"
        "v_smfmac_f32_16x16x64_bf16 v[68:71], v[24:27], a[120:127], v7\n\t"
        "s_waitcnt lgkmcnt(3)\n\t"
        "v_smfmac_f32_16x16x64_bf16 v[64:67], v[28:31], v[112:119], v7\n\t"
        "ds_read_b128 v[112:115], v6 offset:16384\n\t"
        "v_smfmac_f32_16x16x64_bf16 v[68:71], v[28:31], v[176:183], v7\n\t"
        "s_waitcnt lgkmcnt(3)\n\t"
        "s_barrier\n\t"
        "ds_read_b64_tr_b16 v[16:17], v4 offset:24576\n\t"
        "ds_read_b64_tr_b16 v[18:19], v4 offset:26688\n\t"
        "v_smfmac_f32_16x16x64_bf16 v[64:67], v[32:35], a[128:135], v7\n\t"
        "ds_read_b64_tr_b16 v[20:21], v4 offset:25088\n\t"
        "ds_read_b64_tr_b16 v[22:23], v4 offset:27200\n\t"
        "v_smfmac_f32_16x16x64_bf16 v[68:71], v[32:35], a[136:143], v7\n\t"
        "ds_read_b64_tr_b16 v[24:25], v4 offset:25600\n\t"
        "ds_read_b64_tr_b16 v[26:27], v4 offset:27712\n\t"
        "v_smfmac_f32_16x16x64_bf16 v[64:67], v[36:39], a[144:151], v7\n\t"
        "ds_read_b64_tr_b16 v[28:29], v4 offset:26112\n\t"
        "ds_read_b64_tr_b16 v[30:31], v4 offset:28224\n\t"
        "v_smfmac_f32_16x16x64_bf16 v[68:71], v[36:39], a[152:159], v7\n\t"
        "s_waitcnt lgkmcnt(9)\n\t"
        "v_mov_b64 v[32:33], 0\n\t"
        "v_smfmac_f32_16x16x64_bf16 v[64:67], v[40:43], v[120:127], v7\n\t"
        "ds_read_b128 v[116:119], v6 offset:17408\n\t"
        "v_mov_b64 v[34:35], 0\n\t"
        "v_smfmac_f32_16x16x64_bf16 v[68:71], v[40:43], v[184:191], v7\n\t"
        "ds_read_b128 v[120:123], v6 offset:18432\n\t"
        "v_mov_b64 v[36:37], 0\n\t"
        "v_smfmac_f32_16x16x64_bf16 v[64:67], v[44:47], a[160:167], v7\n\t"
        "ds_read_b128 v[124:127], v6 offset:19456\n\t"
        "v_mov_b64 v[38:39], 0\n\t"
        "v_smfmac_f32_16x16x64_bf16 v[68:71], v[44:47], a[168:175], v7\n\t"
        "buffer_store_dword v12, v13, s[8:11], s30 offen sc1\n\t"
        "s_waitcnt lgkmcnt(9)\n\t"
        "v_mov_b64 v[40:41], 0\n\t"
        "v_mfma_f32_16x16x32_bf16 v[48:51], v[72:75], v[16:19], 0\n\t"
        "s_waitcnt lgkmcnt(7)\n\t"
        "v_mov_b64 v[42:43], 0\n\t"
        "v_mov_b64 v[44:45], 0\n\t"
        "v_mfma_f32_16x16x32_bf16 v[48:51], v[76:79], v[20:23], v[48:51]\n\t"
        "s_waitcnt lgkmcnt(5)\n\t"
        "v_add_f32 v64, v64, v66\n\t"
        "v_add_f32 v65, v65, v67\n\t"
        "v_mfma_f32_16x16x32_bf16 v[48:51], v[80:83], v[24:27], v[48:51]\n\t"
        "s_waitcnt lgkmcnt(3)\n\t"
        "v_add_f32 v68, v68, v70\n\t"
        "v_add_f32 v69, v69, v71\n\t"
        "v_mfma_f32_16x16x32_bf16 v[48:51], v[84:87], v[28:31], v[48:51]\n\t"
        "s_cmp_eq_u32 s41, 63\n\t"
        "v_cvt_pk_bf16_f32 v66, v64, v65\n\t"
        "v_cvt_pk_bf16_f32 v67, v68, v69\n\t"
        "v_mfma_f32_16x16x32_bf16 v[52:55], v[88:91], v[16:19], 0\n\t"
        "s_cbranch_scc1 Lh_fin_%=\n\t"
        "v_mov_b32 v68, 0x3f803f80\n\t"
        "v_mov_b32 v69, 0x3f803f80\n\t"
        "v_mfma_f32_16x16x32_bf16 v[52:55], v[92:95], v[20:23], v[52:55]\n\t"
        "ds_read_b128 v[72:75], v6 offset:20480\n\t"
        "v_mov_b32 v70, 0x3f803f80\n\t"
        "v_mfma_f32_16x16x32_bf16 v[52:55], v[96:99], v[24:27], v[52:55]\n\t"
        "ds_read_b128 v[76:79], v6 offset:21504\n\t"
        "v_mov_b32 v71, 0x3f803f80\n\t"
        "v_mfma_f32_16x16x32_bf16 v[52:55], v[100:103], v[28:31], v[52:55]\n\t"
        "ds_read_b128 v[80:83], v6 offset:22528\n\t"
        "v_max3_f32 v10, v48, v49, v50\n\t"
        "v_mfma_f32_16x16x32_bf16 v[56:59], v[16:19], v[16:19], 0\n\t"
        "ds_read_b128 v[84:87], v6 offset:23552\n\t"
        "v_mov_b64 v[46:47], 0\n\t"
        "v_mfma_f32_16x16x32_bf16 v[56:59], v[20:23], v[20:23], v[56:59]\n\t"
        "v_max3_f32 v10, v10, v51, v52\n\t"
        "v_mfma_f32_16x16x32_bf16 v[56:59], v[24:27], v[24:27], v[56:59]\n\t"
        "ds_read_b128 v[88:91], v6 offset:24576\n\t"
        "v_max3_f32 v11, v53, v54, v55\n\t"
        "v_mfma_f32_16x16x32_bf16 v[56:59], v[28:31], v[28:31], v[56:59]\n\t"
        "ds_read_b128 v[92:95], v6 offset:25600\n\t"
        "v_max_f32 v10, v10, v11\n\t"
        "ds_read_b128 v[96:99], v6 offset:26624\n\t"
        "ds_read_b128 v[100:103], v6 offset:27648\n\t"
        "s_nop 3\n\t"
        "v_max3_f32 v10, v10, v56, v57\n\t"
        "v_mov_b32 v11, v10\n\t"
        "s_nop 1\n\t"
        "v_permlane32_swap_b32 v10, v11\n\t"
        "v_max_f32 v10, v10, v11\n\t"
        "v_mov_b32 v11, v10\n\t"
        "s_nop 1\n\t"
        "v_permlane16_swap_b32 v10, v11\n\t"
        "v_max_f32 v10, v10, v11\n\t"
        "v_mul_f32 v10, s52, v10\n\t"
        "v_fma_f32 v56, v56, s52, -v10\n\t"
        "v_fma_f32 v57, v57, s52, -v10\n\t"
        "v_exp_f32 v56, v56\n\t"
        "v_fma_f32 v48, v48, s52, -v10\n\t"
        "v_exp_f32 v57, v57\n\t"
        "v_fma_f32 v49, v49, s52, -v10\n\t"
        "v_exp_f32 v48, v48\n\t"
        "v_cvt_pk_bf16_f32 v12, v56, v57\n\t"
        "v_exp_f32 v49, v49\n\t"
        "v_cvt_pk_bf16_f32 v15, v56, v57\n\t"
        "v_fma_f32 v50, v50, s52, -v10\n\t"
        "v_fma_f32 v51, v51, s52, -v10\n\t"
        "v_mfma_f32_16x16x16_bf16 v[24:27], v[68:69], v[12:13], 0\n\t"
        "s_add_u32 s48, s47, s27\n\t"
        "v_exp_f32 v50, v50\n\t"
        "v_cvt_pk_bf16_f32 v60, v48, v49\n\t"
        "v_exp_f32 v51, v51\n\t"
        "v_mfma_f32_16x16x16_bf16 v[16:19], v[66:67], v[12:13], 0\n\t"
        "buffer_store_dwordx2 v[66:67], v4, s[16:19], s48 offen sc1\n\t"
        "v_fma_f32 v52, v52, s52, -v10\n\t"
        "v_fma_f32 v53, v53, s52, -v10\n\t"
        "v_exp_f32 v52, v52\n\t"
        "v_mfma_f32_16x16x16_bf16 v[20:23], v[66:67], v[14:15], 0\n\t"
        "v_cvt_pk_bf16_f32 v61, v50, v51\n\t"
        "v_exp_f32 v53, v53\n\t"
        "v_fma_f32 v54, v54, s52, -v10\n\t"
        "v_fma_f32 v55, v55, s52, -v10\n\t"
        "v_exp_f32 v54, v54\n\t"
        "v_cvt_pk_bf16_f32 v62, v52, v53\n\t"
        "v_exp_f32 v55, v55\n\t"
        "v_mov_b32 v11, 0\n\t"
        "v_cvt_pk_bf16_f32 v63, v54, v55\n\t"
        "s_nop 1\n\t"
        "v_mfma_f32_16x16x32_bf16 v[24:27], v[68:71], v[60:63], v[24:27]\n\t"
        "v_mfma_f32_16x16x32_bf16 v[16:19], v[104:107], v[60:63], v[16:19]\n\t"
        "buffer_load_dword v15, v13, s[8:11], s72 offen sc1\n\t"
        "v_mov_b64 v[28:29], 0\n\t"
        "v_mov_b64 v[30:31], 0\n\t"
        "v_mfma_f32_16x16x32_bf16 v[20:23], v[108:111], v[60:63], v[20:23]\n\t"
        "v_rcp_f32 v10, v24\n\t"
        "s_nop 2\n\t"
        "v_mul_f32 v48, v16, v10\n\t"
        "v_mul_f32 v49, v17, v10\n\t"
        "v_mul_f32 v50, v18, v10\n\t"
        "v_mul_f32 v51, v19, v10\n\t"
        "v_cvt_pk_bf16_f32 v60, v48, v49\n\t"
        "v_cvt_pk_bf16_f32 v61, v50, v51\n\t"
        "v_mul_f32 v52, v20, v10\n\t"
        "v_mul_f32 v53, v21, v10\n\t"
        "v_mul_f32 v54, v22, v10\n\t"
        "v_mul_f32 v55, v23, v10\n\t"
        "v_cvt_pk_bf16_f32 v62, v52, v53\n\t"
        "v_cvt_pk_bf16_f32 v63, v54, v55\n\t"
        "ds_write_b128 v5, v[60:63]\n\t"
        "v_mov_b64 v[16:17], 0\n\t"
        "v_mov_b64 v[18:19], 0\n\t"
        "v_mov_b64 v[20:21], 0\n\t"
        "v_mov_b64 v[22:23], 0\n\t"
        "v_mov_b64 v[24:25], 0\n\t"
        "v_mov_b64 v[26:27], 0\n\t"
        "s_waitcnt lgkmcnt(0)\n\t"
        "s_barrier\n\t"
        "ds_read_b128 v[48:51], v0 offset:29184\n\t"
        "ds_read_b128 v[52:55], v0 offset:30208\n\t"
        "ds_read_b128 v[104:107], v6 offset:28672\n\t"
        "ds_read_b128 v[108:111], v6 offset:29696\n\t"
        "ds_read_b128 v[64:67], v6 offset:30720\n\t"
        "ds_read_b128 v[68:71], v6 offset:31744\n\t"
        "s_waitcnt lgkmcnt(5)\n\t"
        "v_smfmac_f32_16x16x64_bf16 v[16:19], v[48:51], v[112:119], v7\n\t"
        "s_waitcnt lgkmcnt(4)\n\t"
        "v_smfmac_f32_16x16x64_bf16 v[16:19], v[52:55], v[120:127], v7\n\t"
        "v_bfe_u32 v10, v0, 2, 6\n\t"
        "v_smfmac_f32_16x16x64_bf16 v[20:23], v[48:51], v[72:79], v7\n\t"
        "buffer_load_dword v11, v10, s[8:11], s31 offen sc1\n\t"
        "v_smfmac_f32_16x16x64_bf16 v[20:23], v[52:55], v[80:87], v7\n\t"
        "v_add_f32 v16, v16, v18\n\t"
        "v_smfmac_f32_16x16x64_bf16 v[24:27], v[48:51], v[88:95], v7\n\t"
        "v_add_f32 v17, v17, v19\n\t"
        "v_smfmac_f32_16x16x64_bf16 v[24:27], v[52:55], v[96:103], v7\n\t"
        "s_waitcnt lgkmcnt(2)\n\t"
        "v_add_f32 v20, v20, v22\n\t"
        "v_smfmac_f32_16x16x64_bf16 v[28:31], v[48:51], v[104:111], v7\n\t"
        "s_waitcnt lgkmcnt(0)\n\t"
        "v_add_f32 v21, v21, v23\n\t"
        "v_smfmac_f32_16x16x64_bf16 v[28:31], v[52:55], v[64:71], v7\n\t"
        "v_cvt_pk_bf16_f32 v56, v16, v20\n\t"
        "v_cvt_pk_bf16_f32 v58, v17, v21\n\t"
        "v_smfmac_f32_16x16x64_bf16 v[32:35], v[48:51], v[192:199], v7\n\t"
        "v_add_f32 v24, v24, v26\n\t"
        "v_add_f32 v25, v25, v27\n\t"
        "v_smfmac_f32_16x16x64_bf16 v[32:35], v[52:55], v[200:207], v7\n\t"
        "v_add_f32 v28, v28, v30\n\t"
        "v_add_f32 v29, v29, v31\n\t"
        "v_cvt_pk_bf16_f32 v57, v24, v28\n\t"
        "v_cvt_pk_bf16_f32 v59, v25, v29\n\t"
        "buffer_store_dwordx4 v[56:59], v8, s[8:11], s42 offen sc1\n\t"
        "v_smfmac_f32_16x16x64_bf16 v[36:39], v[48:51], v[208:215], v7\n\t"
        "s_memtime s[60:61]\n\t"
        "v_smfmac_f32_16x16x64_bf16 v[36:39], v[52:55], v[216:223], v7\n\t"
        "v_add_f32 v32, v32, v34\n\t"
        "v_smfmac_f32_16x16x64_bf16 v[40:43], v[48:51], v[224:231], v7\n\t"
        "v_add_f32 v33, v33, v35\n\t"
        "v_smfmac_f32_16x16x64_bf16 v[40:43], v[52:55], v[232:239], v7\n\t"
        "v_add_f32 v36, v36, v38\n\t"
        "v_smfmac_f32_16x16x64_bf16 v[44:47], v[48:51], v[240:247], v7\n\t"
        "v_add_f32 v37, v37, v39\n\t"
        "v_smfmac_f32_16x16x64_bf16 v[44:47], v[52:55], v[248:255], v7\n\t"
        "v_cvt_pk_bf16_f32 v60, v32, v36\n\t"
        "v_cvt_pk_bf16_f32 v62, v33, v37\n\t"
        "v_add_f32 v40, v40, v42\n\t"
        "v_add_f32 v41, v41, v43\n\t"
        "s_nop 3\n\t"
        "v_add_f32 v44, v44, v46\n\t"
        "v_add_f32 v45, v45, v47\n\t"
        "v_cvt_pk_bf16_f32 v61, v40, v44\n\t"
        "v_cvt_pk_bf16_f32 v63, v41, v45\n\t"
        "buffer_store_dwordx4 v[60:63], v8, s[8:11], s43 offen sc1\n\t"
        "ds_read_b64_tr_b16 v[48:49], v9 offset:24576\n\t"
        "ds_read_b64_tr_b16 v[50:51], v9 offset:26688\n\t"
        "s_add_u32 s41, s41, 1\n\t"
        "s_and_b32 s53, s41, 3\n\t"
        "s_lshl_b32 s53, s53, 16\n\t"
        "s_add_u32 s40, s20, s53\n\t"
        "s_xor_b32 s42, s42, 0x80000\n\t"
        "s_xor_b32 s43, s43, 0x80000\n\t"
        "s_waitcnt lgkmcnt(0)\n\t"
        "s_sub_u32 s53, s60, s74\n\t"
        "s_sub_u32 s54, s76, s74\n\t"
        "s_cmp_lg_u32 s49, 0\n\t"
        "s_cselect_b32 s53, s50, s53\n\t"
        "s_cselect_b32 s54, s51, s54\n\t"
        "s_min_u32 s50, s50, s53\n\t"
        "s_min_u32 s51, s51, s54\n\t"
        "v_mov_b32 v12, s41\n\t"
        "s_mov_b32 s53, 0x40000000\n\t"
        "s_cmp_gt_u32 s41, 60\n\t"
        "s_cselect_b32 s53, s30, s53\n\t"
        "s_add_u32 s53, s53, 4096\n\t"
        "buffer_store_dword v12, v13, s[8:11], s53 offen sc1\n\t"
        "buffer_store_dwordx4 v[48:51], v0, s[16:19], s47 offen sc1\n\t"
        "s_add_u32 s47, s47, 0x4000\n\t"
        "s_cmp_lg_u32 s73, s44\n\t"
        "s_cbranch_scc1 Lh_kvl_%=\n\t"
        "s_cmp_eq_u32 s49, 0\n\t"
        "s_cbranch_scc1 Lh_step_%=\n\t"
        "s_waitcnt vmcnt(4)\n\t"
        "Lh_kvr_%=:\n\t"
        "s_branch Lh_step_%=\n\t"
        "Lh_miss_%=:\n\t"
        "v_max3_u32 v10, v16, v17, v18\n\t"
        "v_max3_u32 v10, v10, v19, v20\n\t"
        "v_max3_u32 v10, v10, v21, v22\n\t"
        "v_max3_u32 v10, v10, v23, v24\n\t"
        "v_max3_u32 v10, v10, v25, v26\n\t"
        "v_max3_u32 v10, v10, v27, v28\n\t"
        "v_max3_u32 v10, v10, v29, v30\n\t"
        "v_max3_u32 v10, v10, v31, v32\n\t"
        "v_max3_u32 v10, v10, v33, v34\n\t"
        "v_max3_u32 v10, v10, v35, v36\n\t"
        "v_max3_u32 v10, v10, v37, v38\n\t"
        "v_max3_u32 v10, v10, v39, v40\n\t"
        "v_max3_u32 v10, v10, v41, v42\n\t"
        "v_max3_u32 v10, v10, v43, v44\n\t"
        "v_max3_u32 v10, v10, v45, v46\n\t"
        "v_max_u32 v10, v10, v47\n\t"
        "v_cmp_eq_u32 vcc, -1, v10\n\t"
        "s_cbranch_vccz Lh_cont_%=\n\t"
        "s_add_u32 s49, s49, 2\n\t"
        "s_cmp_gt_u32 s49, 0x40000\n\t"
        "s_cbranch_scc1 Lh_end_%=\n\t"
        "s_waitcnt lgkmcnt(0)\n\t"
        "s_branch Lh_redo_%=\n\t"
        "Lh_late_%=:\n\t"
        "s_or_b32 s49, s49, 1\n\t"
        "s_cmp_eq_u32 s5, 0\n\t"
        "s_cbranch_scc1 Lh_poll_%=\n\t"
        "s_memtime s[74:75]\n\t"
        "buffer_load_dwordx4 v[16:19], v1, s[8:11], s36 offen offset:0 sc1\n\t"
        "buffer_load_dwordx4 v[20:23], v1, s[8:11], s37 offen offset:0 sc1\n\t"
        "s_cmp_ge_u32 s49, 2\n\t"
        "s_cbranch_scc1 Lh_g20_%=\n\t"
        "s_waitcnt vmcnt(6)\n\t"
        "s_barrier\n\t"
        "v_readfirstlane_b32 s53, v15\n\t"
        "s_max_u32 s55, s55, s53\n\t"
        "s_cmp_ge_u32 s55, s56\n\t"
        "s_cselect_b32 s73, s44, 0x40000000\n\t"
        "s_add_u32 s45, s73, 16384\n\t"
        "s_add_u32 s53, s56, 3\n\t"
        "s_min_u32 s53, s53, 64\n\t"
        "s_cmp_lt_u32 s55, s53\n\t"
        "s_cselect_b32 s72, s29, 0x40000000\n\t"
        "s_nop 6\n\t"
        "Lh_g20_%=:\n\t"
        "buffer_load_dwordx4 v[24:27], v1, s[8:11], s38 offen offset:0 sc1\n\t"
        "buffer_load_dwordx4 v[28:31], v1, s[8:11], s39 offen offset:0 sc1\n\t"
        "s_cmp_ge_u32 s49, 2\n\t"
        "s_cbranch_scc1 Lh_g21_%=\n\t"
        "s_and_b32 s53, s28, 0x6000\n\t"
        "v_add_u32 v10, s53, v0\n\t"
        "ds_read_b128 v[72:75], v10 offset:0\n\t"
        "ds_read_b128 v[76:79], v10 offset:1024\n\t"
        "ds_read_b128 v[80:83], v10 offset:2048\n\t"
        "ds_read_b128 v[84:87], v10 offset:3072\n\t"
        "ds_read_b128 v[88:91], v10 offset:4096\n\t"
        "ds_read_b128 v[92:95], v10 offset:5120\n\t"
        "ds_read_b128 v[96:99], v10 offset:6144\n\t"
        "ds_read_b128 v[100:103], v10 offset:7168\n\t"
        "ds_read_b128 v[104:107], v2 offset:0\n\t"
        "ds_read_b128 v[108:111], v2 offset:1024\n\t"
        "s_xor_b32 s28, s28, 0x6000\n\t"
        "s_nop 4\n\t"
        "Lh_g21_%=:\n\t"
        "buffer_load_dwordx4 v[32:35], v1, s[8:11], s36 offen offset:1024 sc1\n\t"
        "buffer_load_dwordx4 v[36:39], v1, s[8:11], s37 offen offset:1024 sc1\n\t"
        "s_cmp_ge_u32 s49, 2\n\t"
        "s_cbranch_scc1 Lh_g22_%=\n\t"
        "s_nop 9\n\t"
        "Lh_g22_%=:\n\t"
        "buffer_load_dwordx4 v[40:43], v1, s[8:11], s38 offen offset:1024 sc1\n\t"
        "buffer_load_dwordx4 v[44:47], v1, s[8:11], s39 offen offset:1024 sc1\n\t"
        "s_mov_b32 m0, s28\n\t"
        "s_andn2_b32 s54, s57, 0x1ffff\n\t"
        "s_and_b32 s53, s7, 0xff\n\t"
        "s_cmp_lt_u32 s49, 2\n\t"
        "s_cselect_b32 s53, 3, s53\n\t"
        "s_cselect_b32 s48, 0, 0x820000\n\t"
        "s_cmp_lt_i32 s57, 0\n\t"
        "s_cselect_b32 s48, s48, 0\n\t"
        "s_cmp_gt_u32 s41, 5\n\t"
        "s_cselect_b32 s48, s48, 0\n\t"
        "s_bfe_u32 s46, s57, 0x60011\n\t"
        "s_cmp_lt_u32 s46, 60\n\t"
        "s_cselect_b32 s48, s48, 0\n\t"
        "s_add_u32 s54, s54, s48\n\t"
        "s_cmp_lt_u32 s41, 2\n\t"
        "s_cselect_b32 s53, 40, s53\n\t"
        "s_add_u32 s53, s53, s60\n\t"
        "s_bfe_u32 s58, s54, 0x80017\n\t"
        "s_bfe_u32 s46, s54, 0x60011\n\t"
        "s_add_u32 s46, s46, s58\n\t"
        "s_mul_i32 s58, s46, s41\n\t"
        "s_add_u32 s58, s58, s34\n\t"
        "s_sub_u32 s53, s53, s58\n\t"
        "s_and_b32 s53, s53, 0x1ffff\n\t"
        "s_or_b32 s53, s53, s54\n\t"
        "v_mov_b32 v10, s53\n\t"
        "v_lshlrev_b32 v11, 4, v0\n\t"
        "s_mov_b64 exec, 0xff\n\t"
        "buffer_store_dword v10, v11, s[8:11], s5 offen sc1\n\t"
        "s_mov_b64 exec, -1\n\t"
        "s_branch Lh_s1_%=\n\t"
        "Lh_cal_%=:\n\t"
        "s_cmp_eq_u32 s50, 0xffff\n\t"
        "s_cbranch_scc1 Lh_calr_%=\n\t"
        "s_cmp_lg_u32 s49, 0\n\t"
        "s_cbranch_scc1 Lh_calr_%=\n\t"
        "s_mov_b32 s4, 1000\n\t"
        "s_cmp_lt_i32 s57, 0\n\t"
        "s_cbranch_scc1 Lh_calr_%=\n\t"
        "s_sub_u32 s53, s59, s78\n\t"
        "s_sub_u32 s54, s74, s79\n\t"
        "v_cvt_f32_u32 v10, s54\n\t"
        "v_cvt_f32_u32 v11, s53\n\t"
        "v_rcp_f32 v10, v10\n\t"
        "s_nop 0\n\t"
        "v_mul_f32 v10, v10, v11\n\t"
        "v_cvt_f32_u32 v11, s50\n\t"
        "v_mul_f32 v11, v11, v10\n\t"
        "v_cvt_u32_f32 v11, v11\n\t"
        "s_nop 0\n\t"
        "v_readfirstlane_b32 s50, v11\n\t"
        "v_cvt_f32_u32 v11, s51\n\t"
        "v_mul_f32 v11, v11, v10\n\t"
        "v_cvt_u32_f32 v11, v11\n\t"
        "s_nop 0\n\t"
        "v_readfirstlane_b32 s51, v11\n\t"
        "s_add_u32 s53, s59, s46\n\t"
        "s_bfe_u32 s54, s7, 0x80008\n\t"
        "s_add_u32 s54, s54, s50\n\t"
        "s_min_u32 s54, s54, 132\n\t"
        "s_lshl_b32 s54, s54, 23\n\t"
        "s_bfe_u32 s48, s7, 0x80010\n\t"
        "s_add_u32 s48, s48, s51\n\t"
        "s_min_u32 s48, s48, 46\n\t"
        "s_lshl_b32 s48, s48, 17\n\t"
        "s_or_b32 s54, s54, s48\n\t"
        "s_or_b32 s54, s54, 0x80000000\n\t"
        "s_add_u32 s48, s41, 1\n\t"
        "s_bfe_u32 s58, s54, 0x80017\n\t"
        "s_bfe_u32 s46, s54, 0x60011\n\t"
        "s_add_u32 s46, s46, s58\n\t"
        "s_mul_i32 s58, s46, s48\n\t"
        "s_add_u32 s58, s58, s34\n\t"
        "s_sub_u32 s53, s53, s58\n\t"
        "s_and_b32 s53, s53, 0x1ffff\n\t"
        "s_or_b32 s53, s53, s54\n\t"
        "v_mov_b32 v10, s53\n\t"
        "v_lshlrev_b32 v11, 4, v0\n\t"
        "s_mov_b64 exec, 0xff\n\t"
        "buffer_store_dword v10, v11, s[8:11], s5 offen sc1\n\t"
        "s_mov_b64 exec, -1\n\t"
        "s_nop 4\n\t"
        "s_branch Lh_calr_%=\n\t"
        "Lh_kvl_%=:\n\t"
        "s_waitcnt vmcnt(0)\n\t"
        "v_mov_b32 v16, v11\n\t"
        "s_cmp_ge_u32 s55, s56\n\t"
        "s_cbranch_scc1 Lh_kv2b_%=\n\t"
        "s_mov_b32 s49, 0\n\t"
        "Lh_kv2a_%=:\n\t"
        "buffer_load_dword v11, v13, s[8:11], s29 offen sc1\n\t"
        "s_waitcnt vmcnt(0)\n\t"
        "v_readfirstlane_b32 s55, v11\n\t"
        "s_add_u32 s49, s49, 1\n\t"
        "s_cmp_gt_u32 s49, 0x40000\n\t"
        "s_cbranch_scc1 Lh_end_%=\n\t"
        "s_sleep 1\n\t"
        "s_cmp_lt_u32 s55, s56\n\t"
        "s_cbranch_scc1 Lh_kv2a_%=\n\t"
        "Lh_kv2b_%=:\n\t"
        "v_mov_b32 v15, s55\n\t"
        "v_mov_b32 v11, v16\n\t"
        "s_mov_b32 s73, s44\n\t"
        "s_add_u32 s45, s44, 16384\n\t"
        "s_mov_b32 m0, s28\n\t"
        "s_nop 0\n\t"
        "buffer_load_dwordx4 v0, s[12:15], s73 offen sc1 lds\n\t"
        "buffer_load_dwordx4 v0, s[12:15], s73 offen offset:1024 sc1 lds\n\t"
        "s_lshl_b32 m0, s25, 10\n\t"
        "s_nop 0\n\t"
        "buffer_load_dwordx4 v0, s[12:15], s45 offen sc1 lds\n\t"
        "buffer_load_dwordx4 v0, s[12:15], s45 offen offset:1024 sc1 lds\n\t"
        "s_waitcnt vmcnt(0)\n\t"
        "s_branch Lh_kvr_%=\n\t"
        "Lh_fin_%=:\n\t"
        "v_mov_b32 v10, 64\n\t"
        "s_add_u32 s53, s30, 4096\n\t"
        "buffer_store_dword v10, v13, s[8:11], s53 offen sc1\n\t"
        "ds_read_b64_tr_b16 v[60:61], v9 offset:24576\n\t"
        "ds_read_b64_tr_b16 v[62:63], v9 offset:26688\n\t"
        "s_add_u32 s48, s47, s27\n\t"
        "buffer_store_dwordx2 v[66:67], v4, s[16:19], s48 offen sc1\n\t"
        "s_waitcnt lgkmcnt(0)\n\t"
        "buffer_store_dwordx4 v[60:63], v0, s[16:19], s47 offen sc1\n\t"
        "s_waitcnt vmcnt(0)\n\t"
        "s_barrier\n\t"
        "buffer_store_dword v10, v13, s[8:11], s30 offen sc1\n\t"
        "Lh_end_%=:\n\t"
        "s_waitcnt vmcnt(0) lgkmcnt(0)\n\t"
        :: [ldsw] "s"(uni(ldsw))
        : "memory", "vcc", "scc", "s4", "s5", "s6", "s7", "s8", "s9", "s10", "s11", "s12", "s13", "s14", "s15", "s16", "s17", "s18", "s19", "s20", "s21", "s22", "s23", "s24", "s25", "s26", "s27", "s28", "s29", "s30", "s31", "s34", "s35", "s36", "s37", "s38", "s39", "s40", "s41", "s42", "s43", "s44", "s45", "s46", "s47", "s48", "s49", "s50", "s51", "s52", "s53", "s54", "s55", "s56", "s57", "s58", "s59", "s60", "s61", "s62", "s63", "s64", "s65", "s66", "s67", "s68", "s69", "s70", "s71", "s72", "s73", "s74", "s75", "s76", "s77", "s78", "s79", "v0", "v1", "v2", "v3", "v4", "v5", "v6", "v7", "v8", "v9", "v10", "v11", "v12", "v13", "v14", "v15", "v16", "v17", "v18", "v19", "v20", "v21", "v22", "v23", "v24", "v25", "v26", "v27", "v28", "v29", "v30", "v31", "v32", "v33", "v34", "v35", "v36", "v37", "v38", "v39", "v40", "v41", "v42", "v43", "v44", "v45", "v46", "v47", "v48", "v49", "v50", "v51", "v52", "v53", "v54", "v55", "v56", "v57", "v58", "v59", "v60", "v61", "v62", "v63", "v64", "v65", "v66", "v67", "v68", "v69", "v70", "v71", "v72", "v73", "v74", "v75", "v76", "v77", "v78", "v79", "v80", "v81", "v82", "v83", "v84", "v85", "v86", "v87", "v88", "v89", "v90", "v91", "v92", "v93", "v94", "v95", "v96", "v97", "v98", "v99", "v100", "v101", "v102", "v103", "v104", "v105", "v106", "v107", "v108", "v109", "v110", "v111", "v112", "v113", "v114", "v115", "v116", "v117", "v118", "v119", "v120", "v121", "v122", "v123", "v124", "v125", "v126", "v127", "v128", "v129", "v130", "v131", "v132", "v133", "v134", "v135", "v136", "v137", "v138", "v139", "v140", "v141", "v142", "v143", "v144", "v145", "v146", "v147", "v148", "v149", "v150", "v151", "v152", "v153", "v154", "v155", "v156", "v157", "v158", "v159", "v160", "v161", "v162", "v163", "v164", "v165", "v166", "v167", "v168", "v169", "v170", "v171", "v172", "v173", "v174", "v175", "v176", "v177", "v178", "v179", "v180", "v181", "v182", "v183", "v184", "v185", "v186", "v187", "v188", "v189", "v190", "v191", "v192", "v193", "v194", "v195", "v196", "v197", "v198", "v199", "v200", "v201", "v202", "v203", "v204", "v205", "v206", "v207", "v208", "v209", "v210", "v211", "v212", "v213", "v214", "v215", "v216", "v217", "v218", "v219", "v220", "v221", "v222", "v223", "v224", "v225", "v226", "v227", "v228", "v229", "v230", "v231", "v232", "v233", "v234", "v235", "v236", "v237", "v238", "v239", "v240", "v241", "v242", "v243", "v244", "v245", "v246", "v247", "v248", "v249", "v250", "v251", "v252", "v253", "v254", "v255", "a0", "a1", "a2", "a3", "a4", "a5", "a6", "a7", "a8", "a9", "a10", "a11", "a12", "a13", "a14", "a15", "a16", "a17", "a18", "a19", "a20", "a21", "a22", "a23", "a24", "a25", "a26", "a27", "a28", "a29", "a30", "a31", "a32", "a33", "a34", "a35", "a36", "a37", "a38", "a39", "a40", "a41", "a42", "a43", "a44", "a45", "a46", "a47", "a48", "a49", "a50", "a51", "a52", "a53", "a54", "a55", "a56", "a57", "a58", "a59", "a60", "a61", "a62", "a63", "a64", "a65", "a66", "a67", "a68", "a69", "a70", "a71", "a72", "a73", "a74", "a75", "a76", "a77", "a78", "a79", "a80", "a81", "a82", "a83", "a84", "a85", "a86", "a87", "a88", "a89", "a90", "a91", "a92", "a93", "a94", "a95", "a96", "a97", "a98", "a99", "a100", "a101", "a102", "a103", "a104", "a105", "a106", "a107", "a108", "a109", "a110", "a111", "a112", "a113", "a114", "a115", "a116", "a117", "a118", "a119", "a120", "a121", "a122", "a123", "a124", "a125", "a126", "a127", "a128", "a129", "a130", "a131", "a132", "a133", "a134", "a135", "a136", "a137", "a138", "a139", "a140", "a141", "a142", "a143", "a144", "a145", "a146", "a147", "a148", "a149", "a150", "a151", "a152", "a153", "a154", "a155", "a156", "a157", "a158", "a159", "a160", "a161", "a162", "a163", "a164", "a165", "a166", "a167", "a168", "a169", "a170", "a171", "a172", "a173", "a174", "a175", "a176", "a177", "a178", "a179", "a180", "a181", "a182", "a183", "a184", "a185", "a186", "a187", "a188", "a189", "a190", "a191", "a192", "a193", "a194", "a195", "a196", "a197", "a198", "a199", "a200", "a201", "a202", "a203", "a204", "a205", "a206", "a207", "a208", "a209", "a210", "a211", "a212", "a213", "a214", "a215", "a216", "a217", "a218", "a219", "a220", "a221", "a222", "a223", "a224", "a225", "a226", "a227", "a228", "a229", "a230", "a231", "a232", "a233", "a234", "a235", "a236", "a237", "a238", "a239", "a240", "a241", "a242", "a243", "a244", "a245", "a246", "a247", "a248", "a249", "a250", "a251", "a252", "a253", "a254", "a255");
}
DI void red_loop(v4u rc, unsigned mo, unsigned ro, unsigned anc, unsigned t0, unsigned dbg, unsigned l16, unsigned pub, v4u sel) {
    asm volatile(
        "v_mov_b32 v0, %[l16]\n\t"
        "v_mov_b32 v1, %[pub]\n\t"
        "v_mov_b32 v36, 0\n\t"
        "v_bfe_u32 v37, v0, 2, 6\n\t"
        "v_mov_b32 v4, %[sel0]\n\t"
        "v_mov_b32 v8, -1\n\t"
        "v_mov_b32 v5, %[sel1]\n\t"
        "v_mov_b32 v9, -1\n\t"
        "v_mov_b32 v6, %[sel2]\n\t"
        "v_mov_b32 v10, -1\n\t"
        "v_mov_b32 v7, %[sel3]\n\t"
        "v_mov_b32 v11, -1\n\t"
        "s_mov_b32 s41, 0\n\t"
        "s_mov_b32 s42, %[ro]\n\t"
        "s_mov_b64 s[46:47], 0\n\t"
        "Lr_step_%=:\n\t"
        "s_mov_b32 s43, 0\n\t"
        "s_and_b32 s53, s41, 3\n\t"
        "s_lshl_b32 s53, s53, 16\n\t"
        "s_add_u32 s45, %[mo], s53\n\t"
        "s_add_u32 s53, s41, 1\n\t"
        "s_and_b32 s53, s53, 3\n\t"
        "s_lshl_b32 s53, s53, 16\n\t"
        "s_add_u32 s40, %[mo], s53\n\t"
        "Lr_anc_%=:\n\t"
        "buffer_load_dword v32, v37, %[rc], %[anc] offen sc1\n\t"
        "s_waitcnt vmcnt(0)\n\t"
        "s_memrealtime s[60:61]\n\t"
        "v_max_u32_dpp v32, v32, v32 row_ror:8 row_mask:0xf bank_mask:0xf\n\t"
        "s_nop 1\n\t"
        "v_max_u32_dpp v32, v32, v32 row_ror:4 row_mask:0xf bank_mask:0xf\n\t"
        "s_nop 1\n\t"
        "v_max_u32_dpp v32, v32, v32 row_ror:2 row_mask:0xf bank_mask:0xf\n\t"
        "s_nop 1\n\t"
        "v_max_u32_dpp v32, v32, v32 row_ror:1 row_mask:0xf bank_mask:0xf\n\t"
        "s_nop 0\n\t"
        "v_readfirstlane_b32 s57, v32\n\t"
        "s_add_u32 s43, s43, 1\n\t"
        "s_cmp_gt_u32 s43, 0x40000\n\t"
        "s_cbranch_scc1 Lr_end_%=\n\t"
        "s_waitcnt lgkmcnt(0)\n\t"
        "s_cmp_eq_u32 s57, 0\n\t"
        "s_cbranch_scc1 Lr_anc_%=\n\t"
        "s_bfe_u32 s54, s57, 0x80017\n\t"
        "s_bfe_u32 s58, s57, 0x60011\n\t"
        "s_add_u32 s58, s58, s54\n\t"
        "s_mul_i32 s58, s58, s41\n\t"
        "s_add_u32 s58, s58, %[t0]\n\t"
        "s_add_u32 s58, s58, s54\n\t"
        "s_and_b32 s59, s57, 0x1ffff\n\t"
        "s_add_u32 s59, s59, s58\n\t"
        "s_sub_u32 s53, s59, s60\n\t"
        "s_cmp_gt_i32 s53, 2000\n\t"
        "s_cbranch_scc1 Lr_poll_%=\n\t"
        "s_cmp_gt_i32 s53, 24\n\t"
        "s_cbranch_scc1 Lr_anc_%=\n\t"
        "s_mov_b32 s43, 0\n\t"
        "Lr_tw_%=:\n\t"
        "s_memrealtime s[60:61]\n\t"
        "s_waitcnt lgkmcnt(0)\n\t"
        "s_sub_u32 s53, s59, s60\n\t"
        "s_cmp_gt_i32 s53, 3\n\t"
        "s_cbranch_scc1 Lr_tw_%=\n\t"
        "s_cmp_lt_i32 s53, 1\n\t"
        "s_cbranch_scc1 Lr_poll_%=\n\t"
        "s_nop 3\n\t"
        "s_cmp_lt_i32 s53, 2\n\t"
        "s_cbranch_scc1 Lr_poll_%=\n\t"
        "s_nop 3\n\t"
        "s_cmp_lt_i32 s53, 3\n\t"
        "s_cbranch_scc1 Lr_poll_%=\n\t"
        "s_nop 3\n\t"
        "Lr_poll_%=:\n\t"
        "buffer_load_dwordx4 v[16:19], v0, %[rc], s42 offen offset:0 sc1\n\t"
        "buffer_load_dwordx4 v[20:23], v0, %[rc], s42 offen offset:1024 sc1\n\t"
        "buffer_load_dwordx4 v[24:27], v0, %[rc], s42 offen offset:2048 sc1\n\t"
        "buffer_load_dwordx4 v[28:31], v0, %[rc], s42 offen offset:3072 sc1\n\t"
        "s_waitcnt vmcnt(3)\n\t"
        "v_mfma_f32_16x16x32_bf16 v[12:15], v[4:7], v[16:19], 0\n\t"
        "s_waitcnt vmcnt(2)\n\t"
        "v_mfma_f32_16x16x32_bf16 v[12:15], v[4:7], v[20:23], v[12:15]\n\t"
        "s_waitcnt vmcnt(1)\n\t"
        "v_mfma_f32_16x16x32_bf16 v[12:15], v[4:7], v[24:27], v[12:15]\n\t"
        "s_waitcnt vmcnt(0)\n\t"
        "v_mfma_f32_16x16x32_bf16 v[12:15], v[4:7], v[28:31], v[12:15]\n\t"
        "s_nop 7\n\t"
        "v_add_f32_dpp v12, v12, v12 row_ror:8 row_mask:0xf bank_mask:0xf\n\t"
        "v_add_f32_dpp v13, v13, v13 row_ror:8 row_mask:0xf bank_mask:0xf\n\t"
        "v_add_f32_dpp v14, v14, v14 row_ror:8 row_mask:0xf bank_mask:0xf\n\t"
        "v_add_f32_dpp v15, v15, v15 row_ror:8 row_mask:0xf bank_mask:0xf\n\t"
        "v_cmp_u_f32 vcc, v12, v12\n\t"
        "v_cvt_pk_bf16_f32 v2, v12, v13\n\t"
        "v_cvt_pk_bf16_f32 v3, v14, v15\n\t"
        "s_cbranch_vccnz Lr_miss_%=\n\t"
        "Lr_got_%=:\n\t"
        "buffer_store_dwordx2 v[2:3], v1, %[rc], s40 offen sc1\n\t"
        "buffer_store_dwordx4 v[8:11], v0, %[rc], s42 offen offset:0 sc1\n\t"
        "buffer_store_dwordx4 v[8:11], v0, %[rc], s42 offen offset:1024 sc1\n\t"
        "buffer_store_dwordx4 v[8:11], v0, %[rc], s42 offen offset:2048 sc1\n\t"
        "buffer_store_dwordx4 v[8:11], v0, %[rc], s42 offen offset:3072 sc1\n\t"
        "buffer_store_dwordx2 v[8:9], v1, %[rc], s45 offen sc1\n\t"
        "s_xor_b32 s42, s42, 0x80000\n\t"
        "s_add_u32 s41, s41, 1\n\t"
        "s_cmp_lt_u32 s41, 63\n\t"
        "s_cbranch_scc1 Lr_step_%=\n\t"
        "s_branch Lr_end_%=\n\t"
        "Lr_miss_%=:\n\t"
        "v_max3_u32 v12, v16, v17, v18\n\t"
        "v_max3_u32 v12, v12, v19, v20\n\t"
        "v_max3_u32 v12, v12, v21, v22\n\t"
        "v_max3_u32 v12, v12, v23, v24\n\t"
        "v_max3_u32 v12, v12, v25, v26\n\t"
        "v_max3_u32 v12, v12, v27, v28\n\t"
        "v_max3_u32 v12, v12, v29, v30\n\t"
        "v_max_u32 v12, v12, v31\n\t"
        "v_cmp_eq_u32 vcc, -1, v12\n\t"
        "s_cbranch_vccz Lr_got_%=\n\t"
        "s_add_u32 s43, s43, 1\n\t"
        "s_cmp_lt_u32 s43, 0x40000\n\t"
        "s_cbranch_scc1 Lr_poll_%=\n\t"
        "Lr_end_%=:\n\t"
        "s_waitcnt vmcnt(0)\n\t"
        :: [rc] "s"(rc), [mo] "s"(uni(mo)), [ro] "s"(uni(ro)), [anc] "s"(uni(anc)), [t0] "s"(uni(t0)), [dbg] "s"(uni(dbg)), [l16] "v"(l16), [pub] "v"(pub), [sel0] "v"(sel.x), [sel1] "v"(sel.y), [sel2] "v"(sel.z), [sel3] "v"(sel.w)
        : "memory", "vcc", "scc", "s40", "s41", "s42", "s43", "s44", "s45", "s46", "s47", "s48", "s49", "s50", "s51", "s52", "s53", "s54", "s55", "s56", "s57", "s58", "s59", "s60", "s61", "v0", "v1", "v2", "v3", "v4", "v5", "v6", "v7", "v8", "v9", "v10", "v11", "v12", "v13", "v14", "v15", "v16", "v17", "v18", "v19", "v20", "v21", "v22", "v23", "v24", "v25", "v26", "v27", "v28", "v29", "v30", "v31", "v32", "v33", "v34", "v35", "v36", "v37", "v38", "v39");
}

struct Params { const bf *x, *mem0, *Wq, *Wk, *Wv, *Wo, *w1, *b1, *w2, *b2; char* ws; bf* out; unsigned tp, i1, lat, pad; };

typedef __attribute__((address_space(4))) const Params* kparg_t;
DI Params fresh_params() {
    kparg_t q = (kparg_t)__builtin_amdgcn_kernarg_segment_ptr();
    asm volatile("" : "+s"(q));
    Params r;
    r.x = q->x; r.mem0 = q->mem0; r.Wq = q->Wq; r.Wk = q->Wk; r.Wv = q->Wv; r.Wo = q->Wo; r.w1 = q->w1; r.b1 = q->b1; r.w2 = q->w2; r.b2 = q->b2; r.ws = q->ws; r.out = q->out;
    r.tp = q->tp; r.i1 = q->i1; r.lat = q->lat; r.pad = q->pad;
    return r;
}
DI unsigned boot_ask(const Params& p) { return __hip_atomic_load((GAS unsigned*)(p.ws + WS_COMM + BT_A + blockIdx.x * 128), RLX_AGENT); }
DI void boot_send(const Params& p, unsigned a, int lane) { if (lane == 0) __hip_atomic_store((GAS unsigned*)(p.ws + WS_COMM + BT_R + blockIdx.x * 128), a + 1, RLX_AGENT); }
DI void boot_wait(const Params& p, unsigned a, int w, int lane) {
    if (w == 0) {
#pragma nounroll
        for (unsigned i = 0; i < SPIN_CAP; ++i) {
            if (uni(boot_ask(p)) == uni(a + 1)) break;
            __builtin_amdgcn_s_sleep(2);
        }
        if (lane == 0) __hip_atomic_store((GAS unsigned*)(p.ws + WS_COMM + BT_D + blockIdx.x * 4), 1u, RLX_AGENT);
    }
    BAR();
}
DI unsigned boot_info(const Params& p) { return poll_ge(p.ws + WS_COMM + FE_INFO + (blockIdx.x & 7) * 128, 1); }
DI void role_setup(const Params& p, int w, int lane) {
    char* c = p.ws + WS_COMM;
    unsigned info = 0;
    TS(p, 4);
    if (w == 0) {
        unsigned lat0 = ~0u, lat1 = ~0u, v0, v1, z = 0;
        unsigned* a0 = (unsigned*)(c + (768u << 10)), *a1 = (unsigned*)(c + (768u << 10) + 8192);
        if (lane == 0) {
            asm volatile("global_atomic_or %0, %2, %4, off sc0 sc1\n\tglobal_atomic_or %1, %3, %4, off sc0 sc1\n\ts_waitcnt vmcnt(0)" : "=&v"(v0), "=&v"(v1) : "v"(a0), "v"(a1), "v"(z) : "memory");
#pragma nounroll
            for (int pass = 0; pass < CAL_PASSES; ++pass) {
                const unsigned long long c0 = __builtin_amdgcn_s_memtime();
                asm volatile("global_atomic_or %0, %1, %2, off sc0 sc1\n\ts_waitcnt vmcnt(0)" : "=v"(v0) : "v"(a0), "v"(z) : "memory");
                const unsigned long long c1 = __builtin_amdgcn_s_memtime();
                asm volatile("global_atomic_or %0, %1, %2, off sc0 sc1\n\ts_waitcnt vmcnt(0)" : "=v"(v1) : "v"(a1), "v"(z) : "memory");
                const unsigned long long c2 = __builtin_amdgcn_s_memtime();
                lat0 = min(lat0, (unsigned)(c1 - c0)); lat1 = min(lat1, (unsigned)(c2 - c1));
            }
        }
        info = (((unsigned)__builtin_amdgcn_s_memrealtime() - (1u << 14)) & ~3u) | 2u | (uni(lat1) < uni(lat0) ? 1u : 0u);
    } else if (w < 3) {
        const int u = (w - 1) * 64 + lane;
        for (unsigned o = FL_KM + u * 128; o < FL_END; o += 128 * 128) __hip_atomic_store((GAS unsigned*)(c + o), 0u, RLX_AGENT);
        for (unsigned o = u; o < 256; o += 128) __hip_atomic_store((GAS unsigned*)(c + (640u << 10) + (o >> 7) * 8192 + ((o >> 4) & 7) * 256 + (o & 15) * 4), 0u, RLX_AGENT);
        VMWAIT(0);
    }
    unsigned ba = 0;
    if (w == 3) { ba = boot_ask(p); boot_send(p, ba, lane); pause_us(2); }
    boot_wait(p, ba, w == 3 ? 0 : 1, lane);
    if (w == 0 && lane < 8) __hip_atomic_store((GAS unsigned*)(c + FE_INFO + lane * 128), info, RLX_AGENT);
    TS(p, 3);
}
DI void role_master(const Params& p, int w, int lane) {
    char* c = p.ws + WS_COMM;
    if (w == 3) {
        const v4u rc = mkrsrc(c, 2u << 20);
        st16((v4u){0u, 0u, 0u, 0u}, rc, lane * 16, BT_D);
#pragma unroll
        for (int i = 0; i < 3; ++i) __hip_atomic_store((GAS unsigned*)(c + FE_XF + (i * 64 + lane) * 128), 0u, RLX_AGENT);
        unsigned r[4], last[4];
#pragma unroll
        for (int k = 0; k < 4; ++k) r[k] = __hip_atomic_load((GAS unsigned*)(c + BT_R + (k * 64 + lane) * 128), RLX_AGENT);
        VMWAIT(0);
        TS(p, 1);
#pragma unroll
        for (int k = 0; k < 4; ++k) last[k] = ~r[k];
#pragma nounroll
        for (unsigned i = 0; i < SPIN_CAP; ++i) {
            bool done = true;
#pragma unroll
            for (int k = 0; k < 4; ++k) {
                if (r[k] != last[k]) __hip_atomic_store((GAS unsigned*)(c + BT_A + (k * 64 + lane) * 128), r[k], RLX_AGENT);
                last[k] = r[k];
            }
#pragma unroll
            for (int k = 0; k < 4; ++k) {
                const int t = k * 64 + lane;
                r[k] = __hip_atomic_load((GAS unsigned*)(c + BT_R + t * 128), RLX_AGENT);
                const unsigned d = __hip_atomic_load((GAS unsigned*)(c + BT_D + t * 4), RLX_AGENT);
                done = done && (d != 0 || (t >= 248 && t < 252 && t != 249));
            }
            if (__all(done)) break;
        }
        TS(p, 2);
    }
}
template <int N> DI void warm_w(const Params& p, int x, int k, int nk, int lane) {
    const unsigned vo = (lane >> 5) * 8192 + (lane & 31) * 64;
    unsigned z[N];
    unsigned long long q0 = (unsigned long long)p.Wk, q1 = (unsigned long long)p.Wv, q2 = (unsigned long long)p.Wq;
    asm volatile("" : "+s"(q0), "+s"(q1), "+s"(q2));
#pragma unroll
    for (int i = 0; i < N; ++i) {
        const int c = i * nk + k, m = c >> 8;
        unsigned long long q = m == 0 ? q0 : q1;
        asm volatile("" : "+s"(q));
        const v4u rw = mkrsrc((const void*)(m >= 2 ? q2 : q), m < 3 ? (4u << 20) : 0u);
        asm volatile("s_nop 4\n\tbuffer_load_dword %0, %1, %2, %3 offen" WARM_POL : "=v"(z[i]) : "v"(vo), "s"(rw), "s"(uni((c & 255) * 16384 + x * 2048)) : "memory");
    }
    VMWAIT(0);
#pragma unroll
    for (int i = 0; i < N; ++i) asm volatile("" : "+v"(z[i]));
}
template <int N> DI void warm_w1(const Params& p, int k, int nk, int lane) {
    const rsrc_t rw1 = mkr(p.w1, 2048u * 1792u * 2u);
    unsigned z[N];
#pragma unroll
    for (int i = 0; i < N; ++i) z[i] = __builtin_amdgcn_raw_buffer_load_b32(rw1, lane * 64, uni((i * nk + k) * (N == 7 ? 4096 : 3584)), 0);
#pragma unroll
    for (int i = 0; i < N; ++i) asm volatile("" :: "v"(z[i]));
}
DI void ln_segment(const Params& p, int s, int w, int lane) {
    v4u xv[8];
#pragma unroll
    for (int i = 0; i < 8; ++i) xv[i] = *(const v4u*)(p.x + (s * 32 + w * 8 + i) * 512 + lane * 8);
#pragma unroll
    for (int i = 0; i < 8; ++i) {
        float v[8]; unpack8(xv[i], v);
        ln8(v);
        ldsw_(((w >> 1) * 16 + (lane >> 2)) * 1040 + ((lane & 3) * 16 + (w & 1) * 8 + i) * 16, repack8(v));
    }
    BAR();
    const v4u rx = mkrsrc(p.ws + WS_XF, 4u << 20);
#pragma unroll
    for (int i = 0; i < 8; ++i) xv[i] = ldsr((w * 8 + i) * 1040 + lane * 16);
#pragma unroll
    for (int i = 0; i < (XFFAR ? 16 : 8); ++i) st16(xv[i & 7], rx, lane * 16 + (i & 7) * 1024, uni(xf_off(s, w, (i >> 3) * 8192)));
}
DI bool wait_64(const char* f, int lane) {
#pragma nounroll
    for (unsigned i = 0; i < SPIN_CAP; ++i) {
        if (__all(__hip_atomic_load((GAS unsigned*)(f + lane * 128), RLX_AGENT) != 0)) { asm volatile("" ::: "memory"); return true; }
        __builtin_amdgcn_s_sleep(2);
    }
    return false;
}
template <bool ISK>
DI unsigned role_p_body(const Params& p, int h, int w, int lane) {
    const int n = lane & 15, g = lane >> 4, q = n >> 2, pp = n & 3;
    v4u W[16][4];
    TS(p, 0);
    unsigned ba = boot_ask(p), fb = 0;
    const v4u rx = mkrsrc(p.ws + WS_XF, 4u << 20);
    bool ok = true;
    {
        const v4u rw = mkrsrc(ISK ? p.Wk : p.Wv, 4u << 20);
        const unsigned stg = w * 32768;
        const int r8 = lane >> 3;
#pragma unroll
        for (int rd = 0; rd < 2; ++rd) {
            for (int i = 0; i < 32; ++i) {
                const int row = i * 8 + r8;
                const int sw = ISK ? ((row >> 1) & 1) * 4 : (((row >> 1) & 1) | (((row >> 3) & 1) << 1)) * 2;
                dma16(rw, (unsigned)(r8 * 8192 + (((lane & 7) ^ sw) * 16)), uni((rd * 256 + i * 8) * 8192 + h * 256 + (w & 1) * 128), uni(stg + i * 1024));
                if (rd == 0 && i == 7 && w == 0) boot_send(p, ba, lane);
                if (rd == 1 && i == 31) {
                    boot_wait(p, ba, w, lane);
                    ok = wait_64(p.ws + WS_COMM + FE_XF, lane);
#pragma unroll
                    for (int k = 0; k < 8; ++k) dma16s(rx, lane * 16 + k * 1024, uni(xf_off(0, w, 0)), uni(131072 + w * 8192 + k * 1024));
                }
            }
            if (rd == 1) fb = (~boot_info(p) & 1) * 8192;
            VMWAIT(0);
#pragma unroll
            for (int ks = 0; ks < 8; ++ks)
#pragma unroll
                for (int ct = 0; ct < 4; ++ct) {
                    unsigned a[2];
#pragma unroll
                    for (int u = 0; u < 2; ++u) {
                        const int row = 8 * g + 4 * u + q;
                        const int sw = ISK ? ((row >> 1) & 1) * 4 : (((row >> 1) & 1) | (((row >> 3) & 1) << 1)) * 2;
                        const int colb = ISK ? (32 * (ct >> 1) + 8 * pp + 4 * (ct & 1)) * 2 : (16 * ct + 4 * pp) * 2;
                        a[u] = stg + (32 * ks + row) * 128 + (((colb >> 4) ^ sw) * 16) + (colb & 8);
                    }
                    W[rd * 8 + ks][ct] = toacc(trf(a[0], a[1]));
                }
            asm volatile("s_waitcnt lgkmcnt(0)" ::: "memory");
        }
    }
    BAR();
    if (!ok) return fb;
    const v4u rkv = mkrsrc(p.ws + WS_KV, 64u << 20);
    unsigned* flag = (unsigned*)(p.ws + WS_COMM + FE_KV + h * 128);
    TS(p, 1);
    v4u Y[4];
    auto put = [&](int sp, int i) {
        const unsigned off = ISK ? ((i >> 1) * 4 + (w & 1) * 2 + (i & 1)) * 1024 : 16384 + ((w & 1) * 4 + i) * 1024;
        st16(Y[i], rkv, lane * 16 + off, uni((h * 64 + sp) * 32768 + (fb ^ KVX ^ ((h & 1) * 8192))));
    };
#pragma unroll
    for (int i = 0; i < 4; ++i) Y[i] = (v4u){0u, 0u, 0u, 0u};
#pragma unroll
    for (int k = 0; k < 8; ++k) dma16s(rx, lane * 16 + k * 1024, uni(xf_off(1, w, fb)), uni(32768 + w * 8192 + k * 1024));
    for (int s = 0; s < 64; ++s) {
        BAR();
        if (w == 0 && lane == 0 && s >= 1) __hip_atomic_store((GAS unsigned*)flag, (unsigned)(s <= 3 ? s : s - 1), RLX_AGENT);
        if (s == 1) TS(p, 2);
        v4f acc[2][4];
        const unsigned xb = (s ? (s & 3) : 4) * 32768 + lane * 16;
        v4u X[16][2];
#pragma unroll
        for (int ks = 0; ks < 2; ++ks) { X[ks][0] = ldsr(xb + ks * 1024); X[ks][1] = ldsr(xb + 16384 + ks * 1024); }
#pragma unroll
        for (int ks = 0; ks < 16; ++ks) {
            if (ks + 2 < 16) { X[ks + 2][0] = ldsr(xb + (ks + 2) * 1024); X[ks + 2][1] = ldsr(xb + 16384 + (ks + 2) * 1024); }
            __builtin_amdgcn_sched_barrier(0);
#pragma unroll
            for (int ct = 0; ct < 4; ++ct) {
                if (ks == 0) { mfa<ISK, true>(acc[0][ct], W[ks][ct], X[ks][0]); mfa<ISK, true>(acc[1][ct], W[ks][ct], X[ks][1]); }
                else { mfa<ISK, false>(acc[0][ct], W[ks][ct], X[ks][0]); mfa<ISK, false>(acc[1][ct], W[ks][ct], X[ks][1]); }
            }
            if (ks < 8) dma16s(rx, lane * 16 + ks * 1024, uni(xf_off(s + 2, w, fb)), uni(((s + 2) & 3) * 32768 + w * 8192 + ks * 1024));
            if ((ks & 1) == 1 && ks < 8) put(s > 3 ? s - 1 : 4096, ks >> 1);
            __builtin_amdgcn_sched_barrier(0);
        }
        MFDONE4(acc[0][0], acc[0][1], acc[0][2], acc[0][3]); MFKEEP4(acc[1][0], acc[1][1], acc[1][2], acc[1][3]);
#pragma unroll
        for (int i = 0; i < 4; ++i) Y[i] = ISK ? pack8(acc[i >> 1][2 * (i & 1)], acc[i >> 1][2 * (i & 1) + 1]) : pack8(acc[0][i], acc[1][i]);
        if (s <= 2) {
#pragma unroll
            for (int i = 0; i < 4; ++i) put(s, i);
        }
        VMWAIT(0);
    }
#pragma unroll
    for (int i = 0; i < 4; ++i) put(63, i);
    VMWAIT(0);
    BAR();
    if (w == 0 && lane == 0) __hip_atomic_store((GAS unsigned*)flag, 64u, RLX_AGENT);
    TS(p, 3);
    return fb;
}

DI void role_qa(const Params& p, unsigned fb, int h, int par, int w, int lane) {
    const int n = lane & 15, g = lane >> 4, q = n >> 2, pp = n & 3;
    v4u WQ[16][2], WO[4][8];
    TS(p, 0);
    {
        const v4u rw = mkrsrc(p.Wq, 4u << 20);
        const int r4 = lane >> 4;
        for (int i = 0; i < 32; ++i) {
            const int row = (w * 32 + i) * 4 + r4;
            dma16(rw, (unsigned)(r4 * 8192 + (((lane & 15) ^ ((row & 3) * 4)) * 16)), uni((w * 32 + i) * 4 * 8192 + h * 256), uni((w * 32 + i) * 1024));
        }
        VMWAIT(0);
        BAR();
#pragma unroll
        for (int ks = 0; ks < 16; ++ks)
#pragma unroll
            for (int ct = 0; ct < 2; ++ct) {
                const unsigned a = (32 * ks + 8 * g + q) * 256 + (((4 * w + pp) ^ (q * 4)) * 16) + ct * 8;
                WQ[ks][ct] = toacc(trf(a, a + 4 * 256));
            }
        BAR();
        const v4u ro = mkrsrc(p.Wo, 4u << 20);
        for (int i = 0; i < 32; ++i)
            dma16(ro, (unsigned)(r4 * 1024 + (((lane & 15) ^ (r4 * 4)) * 16)), uni((h * 128 + i * 4) * 1024 + w * 256), uni(w * 32768 + i * 1024));
        VMWAIT(0);
#pragma unroll
        for (int ks = 0; ks < 4; ++ks)
#pragma unroll
            for (int dt = 0; dt < 8; ++dt) {
                const unsigned a = w * 32768 + (32 * ks + 4 * g + q) * 256 + (((4 * (dt >> 1) + pp) ^ (q * 4)) * 16) + (dt & 1) * 8;
                WO[ks][dt] = toacc(trf(a, a + 16 * 256));
            }
        BAR();
    }
    const v4u rws = mkrsrc(p.ws, (unsigned)WS_PT + (18u << 20));
    const rsrc_t rkm = mkr(p.ws + WS_KM, 32u << 20);
    const char* ctl = p.ws + WS_COMM + fb;
    const int qt = w >> 1, eh = w & 1;
    constexpr unsigned LKV = 65536, LQ = LKV + 32768, LO = LQ + 8192, LY = LO + 8192, LF = LY + 33280;
    constexpr float CSC = 0.08838834764831845f * 1.4426950408889634f;
    auto fetch = [&](int s, int slot, int i) {
        if (i < 8) dma16s(rws, lane * 16 + i * 1024, uni((unsigned)WS_XF + xf_off(s, w, fb)), uni(slot * 32768 + w * 8192 + i * 1024));
        else dma16s(rws, lane * 16 + (i - 8) * 1024, uni((unsigned)WS_KV + (h * 64 + s) * 32768 + (fb ^ KVX ^ ((h & 1) * 8192)) + (w >> 1) * 16384 + (w & 1) * 4096), uni(LKV + slot * 16384 + w * 4096 + (i - 8) * 1024));
    };
    TS(p, 1);
    unsigned kvseen = poll_ge(p.ws + WS_COMM + FE_KV + h * 128, par + 1);
    if (kvseen < (unsigned)par + 1) return;
#pragma unroll
    for (int i = 0; i < 12; ++i) fetch(par, 0, i);
    VMWAIT(0);
    BAR();
    v4u Y[8];
    auto slot = [&](int sp) { return uni((unsigned)WS_PT + pt_slot(sp, fb)); };
    auto put = [&](unsigned so, int i) {
        const int tk = w * 8 + i, sl = (h + tk) & 31;
        st16(Y[i], rws, lane * 16 + tk * 65536 + (sl >> 3) * 16384 + (sl & 7) * 1024, so);
    };
#pragma unroll
    for (int i = 0; i < 8; ++i) Y[i] = (v4u){~0u, ~0u, ~0u, ~0u};
    v4f M0, M1;
#pragma unroll
    for (int r = 0; r < 4; ++r) { M0[r] = (4 * g + r <= 16 * qt + n) ? 0.f : -3.0e38f; M1[r] = (16 + 4 * g + r <= 16 * qt + n) ? 0.f : -3.0e38f; }
    asm volatile("" : "+v"(M0), "+v"(M1));
    for (int it = 0; it < 32; ++it) {
        const int s = 2 * it + par;
        const unsigned pso = it > 0 ? slot(s - 2) : (par ? slot(63) : 0x40000000u);
        unsigned fl = ld_async(ctl + (lane < 32 ? tr_flags(s) + lane * 128 : (lane == 32 ? FE_KV - fb + h * 128 : FL_KM + h * 128)));
        __builtin_amdgcn_sched_barrier(0);
        v4f qa[2][2];
        const unsigned xb = (it & 1) * 32768 + lane * 16;
        v4u X[16][2];
#pragma unroll
        for (int ks = 0; ks < 3; ++ks) { X[ks][0] = ldsr(xb + ks * 1024); X[ks][1] = ldsr(xb + 16384 + ks * 1024); }
#pragma unroll
        for (int ks = 0; ks < 16; ++ks) {
            if (ks + 3 < 16) { X[ks + 3][0] = ldsr(xb + (ks + 3) * 1024); X[ks + 3][1] = ldsr(xb + 16384 + (ks + 3) * 1024); }
            __builtin_amdgcn_sched_barrier(0);
#pragma unroll
            for (int ct = 0; ct < 2; ++ct) {
                if (ks == 0) { mfa<true, true>(qa[0][ct], WQ[ks][ct], X[ks][0]); mfa<true, true>(qa[1][ct], WQ[ks][ct], X[ks][1]); }
                else { mfa<true, false>(qa[0][ct], WQ[ks][ct], X[ks][0]); mfa<true, false>(qa[1][ct], WQ[ks][ct], X[ks][1]); }
            }
            if ((ks & 1) == 0) put(pso, ks >> 1);
            __builtin_amdgcn_sched_barrier(0);
        }
        MFDONE4(qa[0][0], qa[0][1], qa[1][0], qa[1][1]);
        ldsw_(LQ + w * 1024 + lane * 16, pack8(qa[0][0], qa[0][1]));
        ldsw_(LQ + 4096 + w * 1024 + lane * 16, pack8(qa[1][0], qa[1][1]));
        asm volatile("s_waitcnt vmcnt(8)" : "+v"(fl) :: "memory");
        if (w == 0) *(LAS unsigned*)(size_t)(LF + lane * 4) = fl;
        const unsigned mb = uni((h * 64 + s) * 16384 + (fb ^ KMX ^ (((h >> 1) & 1) * 8192)));
        v4u KM[4]; v2u VM[2];
        auto ldkm = [&]() {
#pragma unroll
            for (int ks = 0; ks < 4; ++ks) KM[ks] = __builtin_amdgcn_raw_buffer_load_b128(rkm, lane * 16 + ks * 1024, mb, 16);
#pragma unroll
            for (int i = 0; i < 2; ++i) VM[i] = __builtin_amdgcn_raw_buffer_load_b64(rkm, xslot(lane) * 8 + 4096 + (eh * 2 + i) * 512, mb, 16);
        };
        BAR();
        fl = *(volatile LAS unsigned*)(size_t)(LF + lane * 4);
        const bool early = (unsigned)__builtin_amdgcn_readlane(fl, 33) >= (unsigned)s + 1;
        if (early) ldkm();
        v4u QF[4], KX[2][4], VX[4];
        const unsigned kvb = LKV + (it & 1) * 16384 + lane * 16;
#pragma unroll
        for (int ks = 0; ks < 4; ++ks) { QF[ks] = ldsr(LQ + qt * 4096 + ks * 1024 + lane * 16); KX[0][ks] = ldsr(kvb + ks * 1024); KX[1][ks] = ldsr(kvb + 4096 + ks * 1024); }
#pragma unroll
        for (int i = 0; i < 4; ++i) VX[i] = ldsr(kvb + 8192 + (eh * 4 + i) * 1024);
        __builtin_amdgcn_sched_barrier(0);
        v4f T0 = M0, T1 = M1, Tm;
#pragma unroll
        for (int ks = 0; ks < 4; ++ks) { mfv<false>(T0, KX[0][ks], QF[ks]); mfv<false>(T1, KX[1][ks], QF[ks]); }
        asm volatile("s_nop 7" : "+v"(T0), "+v"(T1));
        kvseen = __builtin_amdgcn_readlane(fl, 32);
        if (it < 31 && kvseen < (unsigned)s + 3) { kvseen = poll_ge(p.ws + WS_COMM + FE_KV + h * 128, s + 3); if (kvseen < (unsigned)s + 3) return; }
        if (s >= 8 && !__all(lane >= 32 || fl >= (unsigned)(s - 7)) && !wait_all_ge(ctl + tr_flags(s), lane, s - 7)) return;
        float sc[10];
#pragma unroll
        for (int r = 0; r < 4; ++r) { sc[r] = T0[r]; sc[4 + r] = T1[r]; }
        float mx = sc[0];
#pragma unroll
        for (int i = 1; i < 8; ++i) mx = fmaxf(mx, sc[i]);
        asm volatile("" : "+v"(mx));
        if (!early) {
            VMWAIT(0);
            BAR();
            if (it > 0 && w == 3 && lane == 0) __hip_atomic_store((GAS unsigned*)(ctl + FL_QA + par * 4096 + h * 128), (unsigned)(s - 1), RLX_AGENT);
            if (w == 0) (void)wait_ge(ctl + (s >= 60 ? FL_KM + 4096 + h * 128 : FL_KM + h * 128), s + 1);
            BAR();
            ldkm();
            if (s >= 60 && __any(max(max(max(umax4(KM[0]), umax4(KM[1])), max(umax4(KM[2]), umax4(KM[3]))), max(max(VM[0].x, VM[0].y), max(VM[1].x, VM[1].y))) == ~0u)) {
                if (!wait_ge(ctl + FL_KM + h * 128, 64)) return;
                ldkm();
            }
        }
        if (it == 31) TS(p, 2);
        mfv<true>(Tm, KM[0], QF[0]);
#pragma unroll
        for (int ks = 1; ks < 4; ++ks) mfv<false>(Tm, KM[ks], QF[ks]);
        asm volatile("s_nop 7" : "+v"(Tm));
        sc[8] = Tm[0]; sc[9] = Tm[1];
        mx = -CSC * xmax_g(fmaxf(mx, fmaxf(sc[8], sc[9])));
        float l = 0;
#pragma unroll
        for (int i = 0; i < 10; ++i) { sc[i] = __builtin_amdgcn_exp2f(__builtin_fmaf(sc[i], CSC, mx)); l += sc[i]; }
        l = __builtin_amdgcn_rcpf(xsum_g(l));
        const v4u PX = {pk2(sc[0] * l, sc[1] * l), pk2(sc[2] * l, sc[3] * l), pk2(sc[4] * l, sc[5] * l), pk2(sc[6] * l, sc[7] * l)};
        const unsigned pm = pk2(sc[8] * l, sc[9] * l);
        v4f O[4];
#pragma unroll
        for (int i = 0; i < 4; ++i) {
            const v4u vm = {VM[i >> 1][i & 1], 0u, 0u, 0u}, pmv = {pm, 0u, 0u, 0u};
            mfv<true>(O[i], VX[i], PX);
            mfv<false>(O[i], vm, pmv);
        }
        MFDONE4(O[0], O[1], O[2], O[3]);
        ldsw_(LO + qt * 4096 + (2 * eh) * 1024 + lane * 16, pack8(O[0], O[1]));
        ldsw_(LO + qt * 4096 + (2 * eh + 1) * 1024 + lane * 16, pack8(O[2], O[3]));
        VMWAIT(0);
        BAR();
        if (early && it > 0 && w == 3 && lane == 0) __hip_atomic_store((GAS unsigned*)(ctl + FL_QA + par * 4096 + h * 128), (unsigned)(s - 1), RLX_AGENT);
        v4u OF[2][4];
#pragma unroll
        for (int i = 0; i < 8; ++i) OF[i >> 2][i & 3] = ldsr(LO + i * 1024 + lane * 16);
        __builtin_amdgcn_sched_barrier(0);
        v4f a[16];
#pragma unroll
        for (int i = 0; i < 18; ++i) {
            if (i < 16) {
                mfa<true, true>(a[i], WO[0][i & 7], OF[i >> 3][0]);
#pragma unroll
                for (int ks = 1; ks < 4; ++ks) mfa<true, false>(a[i], WO[ks][i & 7], OF[i >> 3][ks]);
            }
            if (i < 12) fetch(s + 2, (it + 1) & 1, i);
            if (i >= 3 && (i & 1)) {
                const int pr = (i - 3) >> 1;
                if (i == 17) asm volatile("s_nop 7" : "+v"(a[2 * pr]), "+v"(a[2 * pr + 1])); else asm volatile("" : "+v"(a[2 * pr]), "+v"(a[2 * pr + 1]));
                ldsw_(LY + ((pr >> 2) * 16 + n) * 1040 + w * 256 + (pr & 3) * 64 + g * 16, pack8(a[2 * pr], a[2 * pr + 1]));
            }
            __builtin_amdgcn_sched_barrier(0);
        }
        VMWAIT(4);
        BAR();
#pragma unroll
        for (int i = 0; i < 8; ++i) Y[i] = ldsr(LY + (w * 8 + i) * 1040 + lane * 16);
    }
#pragma unroll
    for (int i = 0; i < 8; ++i) {
        put(slot(62 + par), i);
        if (i == QA_HINT && par && w == 3 && lane == 0) __hip_atomic_store((GAS unsigned*)(ctl + FL_QA + 4096 + h * 128), 64u, RLX_AGENT);
    }
    VMWAIT(0);
    BAR();
    if (w == 3 && lane == 0) __hip_atomic_store((GAS unsigned*)(ctl + FL_QA + par * 4096 + h * 128), (unsigned)(63 + 2 * par), RLX_AGENT);
    TS(p, 3);
}

DI void st8(v2u v, v4u rs, unsigned voff, unsigned soff) {
    asm volatile("buffer_store_dwordx2 %0, %1, %2, %3 offen sc1" :: "v"(v), "v"(voff), "s"(rs), "s"(soff) : "memory");
}
DI void role_tr(const Params& p, unsigned fb, int j, int w, int lane) {
    const int n = lane & 15, g = lane >> 4;
    const rsrc_t rpt = mkr(p.ws + WS_PT, 18u << 20);
    const v4u rh = mkrsrc(p.ws + WS_H, 8u << 20);
    const char* ctl = p.ws + WS_COMM + fb;
    v4u selLo, selHi;
#pragma unroll
    for (int i = 0; i < 4; ++i) {
        selLo[i] = (n == 2 * i ? 0x3f80u : 0u) | (n == 2 * i + 1 ? 0x3f800000u : 0u);
        selHi[i] = (n == 8 + 2 * i ? 0x3f80u : 0u) | (n == 9 + 2 * i ? 0x3f800000u : 0u);
    }
    const int c0 = 128 * (g >> 1) + 8 * n + 4 * (g & 1);
    if (w == 3) st16((v4u){~0u, ~0u, ~0u, ~0u}, rh, lane * 16, uni(hrow(63 * 32 + j, fb) + (4u << 20)));
    for (int s = w; s < 64; s += 4) {
        const int row = s * 32 + j;
        const v2u x0 = *(const v2u*)(p.x + row * 512 + c0), x1 = *(const v2u*)(p.x + row * 512 + 256 + c0);
        if (!wait_all_ge(ctl + FL_QA + (s & 1) * 4096, lane, s + 1)) return;
        if (s == 63) TS(p, 2);
        const unsigned pb = uni(pt_slot(s, fb) + j * 65536);
        v4f a0, a1;
#pragma nounroll
        for (int attempt = 0; attempt < 2; ++attempt) {
            v4u d[8][4];
#pragma unroll
            for (int t = 0; t < 8; ++t)
#pragma unroll
                for (int c = 0; c < 4; ++c) d[t][c] = __builtin_amdgcn_raw_buffer_load_b128(rpt, g * 1024 + n * 16 + c * 256, pb + (t >> 1) * 16384 + (t & 1) * 4096, 16);
            __builtin_amdgcn_sched_barrier(0);
#pragma unroll
            for (int t = 0; t < 8; ++t) {
                if (t == 0) { mfv<true>(a0, selLo, d[t][0]); mfv<true>(a1, selLo, d[t][2]); } else { mfv<false>(a0, selLo, d[t][0]); mfv<false>(a1, selLo, d[t][2]); }
                mfv<false>(a0, selHi, d[t][1]); mfv<false>(a1, selHi, d[t][3]);
            }
            asm volatile("s_nop 7" : "+v"(a0), "+v"(a1));
            if (s != 63 || attempt || !__any(__builtin_isunordered(a0[0], a1[0]))) break;
            if (!wait_all_ge(ctl + FL_QA + 4096, lane, 65)) return;
        }
        if (s == 63 && lane == 0) __hip_atomic_store((GAS unsigned*)(ctl + tr_flags(s) + j * 128), 64u, RLX_AGENT);
        if (s == 63) TS(p, 1);
        float a[8], xv[8];
        unpack8(pack8(a0, a1), a); unpack8((v4u){x0.x, x0.y, x1.x, x1.y}, xv);
#pragma unroll
        for (int i = 0; i < 8; ++i) a[i] += xv[i];
        const v4u hv = repack8(a);
        const unsigned ro = uni(hrow(row, fb));
        st8((v2u){hv.x, hv.y}, rh, c0 * 2, ro); st8((v2u){hv.z, hv.w}, rh, c0 * 2 + 512, ro);
        unpack8(hv, a);
        ln8(a); ln8(a);
        const v4u nv = repack8(a);
        st8((v2u){nv.x, nv.y}, rh, c0 * 2, ro + (4u << 20)); st8((v2u){nv.z, nv.w}, rh, c0 * 2 + 512, ro + (4u << 20));
        VMWAIT(0);
        if (lane == 0) __hip_atomic_store((GAS unsigned*)(ctl + tr_flags(s) + j * 128), (unsigned)(s == 63 ? 65 : s + 1), RLX_AGENT);
        if (j == 0 && s < 60) {
            if (!wait_all_ge(ctl + tr_flags(s), lane, s + 1)) return;
            if (lane == 0) __hip_atomic_store((GAS unsigned*)(ctl + FL_U + 7168 + w * 128), (unsigned)(s + 1), RLX_AGENT);
        }
    }
    TS(p, 3);
}

template <int C> DI unsigned dpp_min(unsigned v) { return min(v, (unsigned)__builtin_amdgcn_update_dpp(0, (int)v, C, 0xf, 0xf, false)); }
DI void role_g1(const Params& p, unsigned fb, int j, int dh, int w, int lane) {
    const int n = lane & 15, g = lane >> 4, q = n >> 2, pp = n & 3, r8 = lane >> 3, dq = dh * 4 + w;
    const v4u rhn = mkrsrc(p.ws + WS_HN, 4u << 20), rw1 = mkrsrc(p.w1, 2048u * 1792u * 2u);
    const char* ctl = p.ws + WS_COMM + fb;
    const unsigned stg = w * 32768;
    v4f acc[4][4];
#pragma unroll
    for (int f2 = 0; f2 < 2; ++f2) {
        float bias[8]; unpack8(*(const v4u*)(p.b1 + 64 * j + 32 * f2 + 8 * g), bias);
#pragma unroll
        for (int dt = 0; dt < 4; ++dt) { acc[2 * f2][dt] = (v4f){bias[0], bias[1], bias[2], bias[3]}; acc[2 * f2 + 1][dt] = (v4f){bias[4], bias[5], bias[6], bias[7]}; }
    }
    const unsigned vh0 = r8 * 1024 + (((lane & 7) ^ (((r8 >> 1) & 1) * 2)) * 16), vh1 = (vh0 ^ 64) + 16384, vw = r8 * 3584 + (((lane & 7) ^ (((r8 >> 1) & 1) * 4)) * 16);
    auto issue_h = [&](int s) {
        const unsigned so = hrow(s * 32, fb) + dq * 128, ld = stg + (s & 3) * 8192;
        dma16s(rhn, vh0, uni(so), uni(ld)); dma16s(rhn, vh1, uni(so), uni(ld + 1024)); dma16s(rhn, vh0, uni(so + 32768), uni(ld + 2048)); dma16s(rhn, vh1, uni(so + 32768), uni(ld + 3072));
    };
    auto issue_w = [&](int s) {
#pragma unroll
        for (int i = 0; i < 4; ++i) dma16(rw1, vw, uni((s * 32 + i * 8) * 3584 + j * 128), uni(stg + (s & 3) * 8192 + 4096 + i * 1024));
    };
    auto first_open = [&]() {
        const unsigned f = __hip_atomic_load((GAS unsigned*)(ctl + FL_U + 7168 + (lane & 3) * 128), RLX_AGENT);
        unsigned m = f ? f + 3 : (unsigned)(lane & 3);
        m = dpp_min<0x122>(m); m = dpp_min<0x121>(m);
        const int r = (int)uni(m);
        asm volatile("" ::: "memory");
        return r;
    };
    int ready = 0, hi = 0;
    bool ok = true;
    const v4u ru = mkrsrc(p.ws + WS_UF, 2u << 20);
#pragma unroll
    for (int i = 0; i < 8; ++i) st16((v4u){~0u, ~0u, ~0u, ~0u}, ru, lane * 16, uni(((dq * 4 + (i >> 1)) * 56 + 2 * j + (i & 1)) * 1024));
    issue_w(0);
    for (int s = 0; s < 64; ++s) {
        if (hi <= s) {
            if (s >= ready) {
                if (s < 60) { for (unsigned i = 0; ok; ++i) { ready = first_open(); if (ready > s) break; if (i > SPIN_CAP) ok = false; __builtin_amdgcn_s_sleep(4); } }
                else {
                    if (w == 0) {
                        int r = 0;
                        for (unsigned i = 0; i < SPIN_CAP; ++i) {
                            const unsigned f0 = __hip_atomic_load((GAS unsigned*)(ctl + FL_TR + lane * 128), RLX_AGENT), f1 = __hip_atomic_load((GAS unsigned*)(ctl + FL_TR + 16384 + lane * 128), RLX_AGENT);
                            const unsigned c = lane >> 5;
                            const unsigned long long b0 = __ballot(f0 >= 61 + c), b1 = __ballot(f1 >= 63 + c);
                            r = (unsigned)b0 != ~0u ? 60 : (b0 >> 32) != 0xffffffffull ? 61 : (unsigned)b1 != ~0u ? 62 : (b1 >> 32) != 0xffffffffull ? 63 : 64;
                            if (r > s) break;
                            __builtin_amdgcn_s_sleep(2);
                        }
                        *(volatile LAS int*)(size_t)(131072 + (s & 3) * 4) = max(r, s + 1);
                    }
                    BAR();
                    ready = uni(*(volatile LAS int*)(size_t)(131072 + (s & 3) * 4));
                }
            }
            issue_h(s); issue_w(s + 1); hi = s + 1;
        }
        while (hi < ready && hi < s + 3) { issue_h(hi); issue_w(hi + 1); ++hi; }
        if (s == 63) TS(p, 4);
        if (hi == s + 1) VMWAIT(4); else if (hi == s + 2) VMWAIT(12); else VMWAIT(20);
        const unsigned sk = stg + (s & 3) * 8192;
        if (s == 63) {
            unsigned m = 0;
#pragma unroll
            for (int i = 0; i < 4; ++i) m = max(m, umax4(*(volatile LAS v4u*)(size_t)(sk + i * 1024 + lane * 16)));
            if (__any(m == ~0u)) { ok = ok && wait_all_ge(ctl + tr_flags(s), lane, 65); issue_h(s); VMWAIT(0); }
        }
        v4u A[4], B[4];
#pragma unroll
        for (int ft = 0; ft < 4; ++ft) {
            const unsigned a = sk + 4096 + (8 * g + q) * 128 + (((4 * (ft >> 1) + pp) ^ (((q >> 1) & 1) * 4)) * 16) + (ft & 1) * 8;
            A[ft] = trf(a, a + 4 * 128);
        }
#pragma unroll
        for (int dt = 0; dt < 4; ++dt) {
            const unsigned a = sk + (8 * g + q) * 128 + (((2 * dt + (pp >> 1)) ^ ((((q >> 1) & 1) | ((g & 1) << 1)) * 2)) * 16) + (pp & 1) * 8;
            B[dt] = trf(a, a + 4 * 128);
        }
        __builtin_amdgcn_sched_barrier(0);
#pragma unroll
        for (int dt = 0; dt < 4; ++dt)
#pragma unroll
            for (int ft = 0; ft < 4; ++ft) mfv<false>(acc[ft][dt], A[ft], B[dt]);
        asm volatile("" ::: "memory");
    }
    TS(p, 5);
    MFDONE4(acc[0][3], acc[1][3], acc[2][3], acc[3][3]);
#pragma unroll
    for (int dt = 0; dt < 4; ++dt) {
        MFKEEP4(acc[0][dt], acc[1][dt], acc[2][dt], acc[3][dt]);
#pragma unroll
        for (int f2 = 0; f2 < 2; ++f2) {
            float y[8];
#pragma unroll
            for (int i = 0; i < 8; ++i) {
                const float v = acc[2 * f2 + (i >> 2)][dt][i & 3];
                const float z = v * (-2.302208198f - 0.1029432397f * v * v);
                y[i] = v * __builtin_amdgcn_rcpf(1.f + __builtin_amdgcn_exp2f(z));
            }
            st16(repack8(y), ru, lane * 16, uni(((dq * 4 + dt) * 56 + 2 * j + f2) * 1024));
        }
        if (dt == G1_HINT && w == 0 && lane == 0) __hip_atomic_store((GAS unsigned*)(ctl + FL_U + (dh * 28 + j) * 128), 1u, RLX_AGENT);
    }
    VMWAIT(0);
    BAR();
    if (w == 0 && lane == 0) __hip_atomic_store((GAS unsigned*)(ctl + FL_U + (dh * 28 + j) * 128), 2u, RLX_AGENT);
    TS(p, 6);
}

DI void g2_load(const Params& p, int sb, int w, int lane, v4u (&B)[14][4]) {
    const int n = lane & 15, g = lane >> 4, q = n >> 2, pp = n & 3, r8 = lane >> 3;
    const v4u rw = mkrsrc(p.w2, 1792u * 2048u * 2u);
    const unsigned stg = w * 32768;
    const unsigned vo0 = r8 * 4096 + (((lane & 7) ^ (((r8 >> 1) & 1) * 2)) * 16), vo1 = (vo0 ^ 64) + 8 * 4096;
#pragma unroll
    for (int rd = 0; rd < 2; ++rd) {
        for (int i = 0; i < 14; ++i) {
            const unsigned so = uni(((w * 14 + rd * 7) * 32 + i * 16) * 4096 + sb * 128);
            dma16(rw, vo0, so, uni(stg + i * 2048)); dma16(rw, vo1, so, uni(stg + i * 2048 + 1024));
        }
        VMWAIT(0);
#pragma unroll
        for (int ks = 0; ks < 7; ++ks)
#pragma unroll
            for (int st = 0; st < 4; ++st) {
                const unsigned a = stg + (32 * ks + 8 * g + q) * 128 + (((2 * st + (pp >> 1)) ^ ((((q >> 1) & 1) | ((g & 1) << 1)) * 2)) * 16) + (pp & 1) * 8;
                B[rd * 7 + ks][st] = toacc(trf(a, a + 4 * 128));
            }
        asm volatile("s_waitcnt lgkmcnt(0)" ::: "memory");
    }
}
DI void g2_run(const Params& p, unsigned fb, int sb, int grp, int w, int lane, v4u (&B)[14][4]) {
    const int n = lane & 15, g = lane >> 4;
    const int t0 = (grp * 16) / 3, nt = ((grp + 1) * 16) / 3 - t0;
    const rsrc_t ru = mkr(p.ws + WS_UF, 56u * 32u * 1024u), rh = mkr(p.ws + WS_H, 4u << 20), ro = mkr(p.out, 2u << 20);
    const char* ctl = p.ws + WS_COMM + fb;
    const int srow = sb * 64 + w * 16 + n;
    const float bias = b2f(p.b2[srow]);
    {
        TS(p, 4);
        const char* f = ctl + FL_U + ((((lane & 8) ? t0 + 5 : t0) >> 4) * 28 + 7 * w + min(lane & 7, 6)) * 128;
        const unsigned v5 = nt == 6 ? lane * 16 + 5 * 57344 : 0x40000000u;
        const unsigned ub = uni((t0 * 56 + w * 14) * 1024);
        v4f acc[6][4];
        v2u hv[6];
#pragma nounroll
        for (unsigned attempt = 1; attempt <= 2; ++attempt) {
            for (unsigned i = 0; i < SPIN_CAP; ++i) {
                if (__all(__hip_atomic_load((GAS unsigned*)f, RLX_AGENT) >= attempt)) break;
                __builtin_amdgcn_s_sleep(3);
            }
            asm volatile("" ::: "memory");
            TS(p, 5);
            constexpr int PF = G2PF;
            v4u A[14][6];
#pragma unroll
            for (int ks = 0; ks < PF; ++ks)
#pragma unroll
                for (int dt = 0; dt < 6; ++dt) A[ks][dt] = __builtin_amdgcn_raw_buffer_load_b128(ru, dt == 5 ? v5 : lane * 16 + dt * 57344, ub + ks * 1024, 16);
            __builtin_amdgcn_sched_barrier(0);
#pragma unroll
            for (int ks = 0; ks < 14; ++ks) {
#pragma unroll
                for (int dt = 0; dt < 6; ++dt) {
#pragma unroll
                    for (int st = 0; st < 4; ++st) { if (ks == 0) mfa<false, true>(acc[dt][st], B[ks][st], A[ks][dt]); else mfa<false, false>(acc[dt][st], B[ks][st], A[ks][dt]); }
                    if (ks + PF < 14) A[ks + PF][dt] = __builtin_amdgcn_raw_buffer_load_b128(ru, dt == 5 ? v5 : lane * 16 + dt * 57344, ub + (ks + PF) * 1024, 16);
                    __builtin_amdgcn_sched_barrier(0);
                }
            }
            MFDONE4(acc[5][0], acc[5][1], acc[5][2], acc[5][3]);
            v4f c = acc[5][0];
#pragma unroll
            for (int dt = 0; dt < 5; ++dt) { MFKEEP4(acc[dt][0], acc[dt][1], acc[dt][2], acc[dt][3]); c += acc[dt][0]; }
            if (!__any(__builtin_isunordered(c[0] + c[1], c[2] + c[3]))) break;
        }
        TS(p, 7);
        if (sb == 31) (void)wait_all_ge(ctl + tr_flags(63), lane, 65);
#pragma unroll
        for (int dt = 0; dt < 6; ++dt) hv[dt] = __builtin_amdgcn_raw_buffer_load_b64(rh, hrow(srow, fb) + g * 8 + dt * 32, uni(t0 * 32), 16);
#pragma unroll
        for (int dt = 0; dt < 6; ++dt) {
            MFKEEP4(acc[dt][0], acc[dt][1], acc[dt][2], acc[dt][3]);
#pragma unroll
            for (int st = 0; st < 4; ++st) *(LAS v4f*)(size_t)(w * 32768 + (st * 6 + dt) * 1024 + lane * 16) = acc[dt][st];
        }
        BAR();
        v4f y[6][4];
#pragma unroll
        for (int dt = 0; dt < 6; ++dt)
#pragma unroll
            for (int k = 0; k < 4; ++k) y[dt][k] = *(LAS v4f*)(size_t)(k * 32768 + (w * 6 + dt) * 1024 + lane * 16);
        __builtin_amdgcn_sched_barrier(0);
#pragma unroll
        for (int dt = 0; dt < 6; ++dt) {
            const v4f z = (y[dt][0] + y[dt][1]) + (y[dt][2] + y[dt][3]);
            const float r0 = z[0] + bias + lo2f(hv[dt].x), r1 = z[1] + bias + hi2f(hv[dt].x), r2 = z[2] + bias + lo2f(hv[dt].y), r3 = z[3] + bias + hi2f(hv[dt].y);
            __builtin_amdgcn_raw_buffer_store_b64((v2u){pk2(r0, r1), pk2(r2, r3)}, ro, srow * 1024 + g * 8 + dt * 32, uni(t0 * 32 + (dt < nt ? 0u : 0x40000000u)), 0);
        }
    }
    TS(p, 6);
}

extern "C" __global__ void __launch_bounds__(256, 1) mk_main(Params p0) {
    extern __shared__ __attribute__((aligned(16))) unsigned lds[];
    const int b = blockIdx.x;
    const int id = (b >> 3) * 4 + (b & 3);
    const int w = __builtin_amdgcn_readfirstlane(threadIdx.x >> 6);
    if (CODEWARM) {
        unsigned plo, phi;
        asm volatile("s_getpc_b64 s[40:41]\n\ts_add_u32 %0, s40, mk_main@rel32@lo+4\n\ts_addc_u32 %1, s41, mk_main@rel32@hi+12" : "=s"(plo), "=s"(phi) :: "scc", "s40", "s41");
        const v4u rcode = {plo, phi, 73728u, 0x00020000u};
        if (w == 3) asm volatile("s_mov_b32 m0, %2\n\ts_nop 4\n\tbuffer_load_dword %0, %1, 0 offen lds" :: "v"((31 - (b >> 3)) * 4096 + lane_id() * 64), "s"(rcode), "s"(160 * 1024 - 512) : "memory");
    }
    if (!(b & 4) && id >= 124) { if (id == 124) role_master(fresh_params(), w, lane_id()); if (id == 125) role_setup(fresh_params(), w, lane_id()); return; }
    if ((b & 4) && id >= 96) {
        unsigned fb;
        if (w < 2) fb = role_p_body<true>(fresh_params(), (id & 3) * 8 + ((id - 96) >> 2), w, lane_id());
        else fb = role_p_body<false>(fresh_params(), (id & 3) * 8 + ((id - 96) >> 2), w, lane_id());
        if (id < 124) role_g1(fresh_params(), fb, id - 96, 1, w, lane_id());
        return;
    }
    unsigned info = 0;
    if ((b & 4) || id >= 32) {
        const Params p = fresh_params();
        const int lane = lane_id();
        unsigned ba = 0;
        if (w == 0) { ba = boot_ask(p); boot_send(p, ba, lane); }
        if ((b & 4) && id < 64) {
            ln_segment(p, (id >> 2) + (id & 3) * 16, w, lane);
            VMWAIT(0);
        } else {
            pause_us(WARM_DELAY);
            if (b & 4) warm_w<16>(p, id & 3, ((id - 64) >> 2) * 4 + w, 32, lane);
            else warm_w<9>(p, id & 3, ((id - 32) >> 2) * 4 + w, 92, lane);
        }
        boot_wait(p, ba, w, lane);
        if ((b & 4) && id < 64 && w == 0 && lane == 0) __hip_atomic_store((GAS unsigned*)(p.ws + WS_COMM + FE_XF + id * 128), 1u, RLX_AGENT);
        if ((b & 4) && id < 64) warm_w1<7>(p, id * 4 + w, 256, lane);
        info = boot_info(p);
    }
    unsigned fb = (~info & 1) * 8192;
    if ((b & 4) && id >= 64) {
        const Params p = fresh_params();
        const int lane = lane_id();
        v4u B[14][4];
        pause_us(TR_DELAY);
        g2_load(p, id - 64, w, lane, B);
        role_tr(p, fb, id - 64, w, lane);
        g2_run(p, fb, id - 64, 5, w, lane, B);
        return;
    }
    if (id >= 96) { role_g1(fresh_params(), fb, id - 96, 0, w, lane_id()); return; }
    if (b & 4) {
        role_qa(fresh_params(), fb, (id & 3) * 8 + (id >> 3), (id >> 2) & 1, w, lane_id());
    } else
    if (id < 32) {
        const Params p = fresh_params();
        const int h = (id & 3) * 8 + (id >> 2), lane = lane_id(), n = lane & 15, g = lane >> 4, q = n >> 2, pp = n & 3;
        TS(p, 0);
        auto rho = [](int i, int hf) { return 4 * (i >> 1) + 2 * hf + (i & 1); };
        const unsigned ldsw = 0x8000 + w * 0x8000;
        v4u c0, c1, c2, c3;
        c0.x = lane * 16;
        c0.y = ((g >> 1) * 4 + (n >> 2)) * 128 + (g & 1) * 64 + (n & 3) * 16;
        c0.z = w * 2048 + lane * 16;
        c0.w = 0x6000 + w * 512 + ((n >> 2) & 1) * 2112 + ((n & 3) + 4 * (n >> 3) + 8 * g) * 8;
        c1.x = xslot(lane) * 8;
        { const int i = 2 * (n >> 2) + (n & 1), ga = 2 * (w & 1) + (g >> 1), hf = g & 1;
          c1.y = (n & 2) ? 0x7200 + (w >> 1) * 1024 + (ga * 16 + rho(i, hf)) * 16 : 0x7A00 + (lane ^ (hf ? 0 : 2)) * 16; }
        c1.z = ldsw + lane * 16;
        c1.w = (lane & 2) ? 0xEEEEu : 0x4444u;
        c2.x = (w * 8 + (n >> 3) * 2 + (g >> 1)) * 16384 + (g & 1) * 4096 + h * 128 + (n & 7) * 16;
        c2.y = w * 512 + xslot(lane) * 8;
        { const int lr = lane >> 2, row = lr ^ (((lr >> 3) & 1) << 1);
          c2.z = (16 * (row >> 3) + 8 * ((row >> 2) & 1) + (row & 3)) * 8192 + (((lane & 3) ^ (((row >> 2) & 1) << 1)) * 16); }
        c2.w = (16 * (g & 1) + 8 * (g >> 1)) * 1024 + n * 16;
#pragma unroll
        for (int i = 0; i < 4; ++i) {
            const int t = i >> 1, u = i & 1, row = 16 * (g >> 1) + 4 * (g & 1) + 2 * u + (q & 1) + 8 * (q >> 1), lr = row ^ (((row >> 3) & 1) << 1);
            c3[i] = ldsw + lr * 64 + (((2 * t + (pp >> 1)) ^ (((row >> 2) & 1) << 1)) * 16) + (pp & 1) * 8;
        }
        ldsw_(ldsw + lane * 16, c0); ldsw_(ldsw + 1024 + lane * 16, c1); ldsw_(ldsw + 2048 + lane * 16, c2); ldsw_(ldsw + 3072 + lane * 16, c3);
        {
            const unsigned nb = 0, fb = 8192, t0 = w == 0 ? BT_A + b * 128 : 0u;
            const unsigned long long pp_[7] = {(unsigned long long)(p.ws + WS_COMM), (unsigned long long)(p.ws + WS_KV), (unsigned long long)(p.ws + WS_KM),
                                               (unsigned long long)p.Wq, (unsigned long long)p.Wk, (unsigned long long)p.Wv, (unsigned long long)p.Wo};
            const unsigned sz_[7] = {2u << 20, 64u << 20, 32u << 20, 4u << 20, 4u << 20, 4u << 20, 4u << 20};
            const unsigned sc_[17] = {(b & 3) * 16384 + nb * 8192, (1u << 20) + nb * 8192, h * (2u << 20) + (fb ^ KVX ^ ((h & 1) * 8192)) + w * 2048, h * 256u + w * 64, h * 131072u + w * 256, 2u * w,
                                      h * (1u << 20) + (fb ^ KMX ^ (((h >> 1) & 1) * 8192)) + w * 1024, 4096u - w * 512, 0x2000u + w * 2048,
                                      FE_KV + h * 128, w == 0 ? FL_KM + fb + h * 128 : 0x40000000u, (640u << 10) + nb * 8192 + (b & 3) * 256, t0, (id < 4 && w == 0) ? (640u << 10) + nb * 8192 + id * 4 : 0u, p.pad >= 1000u ? (p.i1 << 23) | (p.tp << 17) | (p.pad >= 2000u ? 0x80000000u : 0u) : (123u << 23) | (41u << 17), (p.lat & 255u) | (p.i1 << 8) | (p.tp << 16), (id < 4 && w == 0) ? p.pad : 1000u};
            unsigned val = 0;
#pragma unroll
            for (int i = 0; i < 7; ++i) { val = lane == 3 * i ? (unsigned)pp_[i] : val; val = lane == 3 * i + 1 ? (unsigned)(pp_[i] >> 32) : val; val = lane == 3 * i + 2 ? sz_[i] : val; }
#pragma unroll
            for (int i = 0; i < 17; ++i) val = lane == 21 + i ? sc_[i] : val;
            *(LAS unsigned*)(size_t)(ldsw + 4096 + lane * 4) = val;
        }
        asm volatile("s_waitcnt lgkmcnt(0)" ::: "memory");
        head_loop(ldsw);
        fb = (~uni(*(volatile LAS unsigned*)(size_t)0x7F00) & 1) * 8192;
    } else if (w == 0) {
        const unsigned r = id - 32;
        const Params p = fresh_params();
        const v4u rc = mkrsrc(p.ws + WS_COMM, 2u << 20);
        const int lane = lane_id(), n = lane & 15, g = lane >> 4;
        v4u sel;
        for (int j = 0; j < 4; ++j) sel[j] = ((n & 7) == 2 * j ? 0x3f80u : 0u) | ((n & 7) == 2 * j + 1 ? 0x3f800000u : 0u);
        const unsigned pub = ((n >> 3) + 2 * (g >> 1)) * 16384 + ((n & 7) >> 1) * 32 + (g & 1) * 16 + (n & 1) * 8;
        const unsigned nb = info & 1, t0 = info + ((r >> 3) & 1) * (p.lat >> 24);
        {
            const v4u ones = {~0u, ~0u, ~0u, ~0u};
            const unsigned ib = (1u << 20) + (r >> 1) * 16384 + nb * 8192 + (r & 1) * 4096;
#pragma unroll
            for (int i = 0; i < 8; ++i) st16(ones, rc, lane * 16 + (i & 3) * 1024, uni(ib + (i >> 2) * 0x80000));
#pragma unroll
            for (int i = 1; i < 4; ++i) st8((v2u){~0u, ~0u}, rc, pub, uni(nb * 8192 + r * 128 + i * 65536));
            st8(*(const v2u*)(p.mem0 + (2 * (r & 3) + (g & 1)) * 512 + (r >> 4) * 128 + ((r >> 2) & 1) * 64 + 8 * (n & 7) + 4 * ((r >> 3) & 1)), rc, pub, uni(nb * 8192 + r * 128));
            VMWAIT(0);
            if (lane == 0) __hip_atomic_store((GAS unsigned*)(p.ws + WS_COMM + FE_RD + r * 128), info, RLX_AGENT);
        }
        red_loop(rc, nb * 8192 + r * 128, (1u << 20) + (r >> 1) * 16384 + nb * 8192 + (r & 1) * 4096, (640u << 10) + nb * 8192 + ((b & 3) + 4) * 256, t0, (512u << 10) + r * 8, lane * 16, pub, sel);
    }
    BAR();
    {
        const Params p = fresh_params();
        const int lane = lane_id();
        const int grp = (b & 4) ? 3 + (id & 1) : (id >> 5), sb = (b & 4) ? (id >> 1) : (id & 31);
        v4u B[14][4];
        g2_load(p, sb, w, lane, B);
        g2_run(p, fb, sb, grp, w, lane, B);
    }
}

extern "C" void kernel_launch(void* const* d_in, const int* in_sizes, int n_in, void* d_out, int out_size, void* d_ws, size_t ws_size, hipStream_t stream) {
    Params p;
    p.x = (const bf*)d_in[0]; p.mem0 = (const bf*)d_in[1]; p.Wq = (const bf*)d_in[2]; p.Wk = (const bf*)d_in[3]; p.Wv = (const bf*)d_in[4]; p.Wo = (const bf*)d_in[5];
    p.w1 = (const bf*)d_in[8]; p.b1 = (const bf*)d_in[9]; p.w2 = (const bf*)d_in[10]; p.b2 = (const bf*)d_in[11];
    p.ws = (char*)(((unsigned long long)d_ws + (2u << 20) - 1) & ~(unsigned long long)((2u << 20) - 1));
    p.out = (bf*)d_out;
    unsigned db = 8;
    p.i1 = 120; p.tp = 39; p.lat = 36; p.pad = 2000;
    p.lat |= db << 24;
    (void)hipFuncSetAttribute((const void*)mk_main, hipFuncAttributeMaxDynamicSharedMemorySize, 160 * 1024);
    mk_main<<<256, 256, 160 * 1024, stream>>>(p);
}
```
